```python
import torch

def _softmax(x, axis=-1):
    return torch.softmax(x, dim=axis)
def _gelu(x):
    return torch.nn.functional.gelu(x, approximate="tanh")
def _silu(x):
    return torch.nn.functional.silu(x)
def _sigmoid(x):
    return torch.sigmoid(x)
def _layernorm(x, axis=-1, eps=1e-5):
    m = x.mean(dim=axis, keepdim=True)
    v = x.var(dim=axis, keepdim=True, unbiased=False)
    return (x - m) / torch.sqrt(v + eps)
def _scatter_add(tgt, idx, src, axis=0):
    return tgt.clone().index_add_(axis, idx, src)
def _segment_sum(src, seg, n):
    out = torch.zeros((n,) + src.shape[1:], dtype=src.dtype)
    return out.index_add_(0, seg, src)
def _pad(x, pw):
    flat = tuple(v for pair in reversed(pw) for v in pair)
    return torch.nn.functional.pad(x, flat)

kernel_name = 'sequential_preLN__sliding_block__mixer_channel_mlp__prompt'

def setup_inputs():

    _w = (lambda a: (lambda t: t.to(torch.bfloat16) if t.is_floating_point() else t)(torch.as_tensor(a)))
    def _A_setup():
        B, S, D = 1, 2048, 1024
        return {
            "x": _w((lambda *s: torch.randn(*s, dtype=torch.bfloat16) * 0.05)(B, S, D)),
            "wqkv": _w((lambda *s: torch.randn(*s, dtype=torch.bfloat16) * 0.05)(D, 3 * D)),
            "wo": _w((lambda *s: torch.randn(*s, dtype=torch.bfloat16) * 0.05)(D, D)),
        }
    _A_in = _A_setup()

    def _B_setup():
        B, S, D, F = 1, 2048, 1024, 3584
        return {
            "x": _w((lambda *s: torch.randn(*s, dtype=torch.bfloat16) * 0.05)(B, S, D)),
            "w1": _w((lambda *s: torch.randn(*s, dtype=torch.bfloat16) * 3.2)(D, F) * (D ** -0.5)),
            "b1": _w((lambda *s: torch.randn(*s, dtype=torch.bfloat16) * 1.6)(F)),
            "w2": _w((lambda *s: torch.randn(*s, dtype=torch.bfloat16) * 0.4)(F, D) * (F ** -0.5)),
            "b2": _w((lambda *s: torch.randn(*s, dtype=torch.bfloat16) * 0.8)(D)),
        }
    _B_in = _B_setup()

    return {
        "A_x": _A_in["x"],
        "A_wqkv": _A_in["wqkv"],
        "A_wo": _A_in["wo"],
        "B_x": _B_in["x"],
        "B_w1": _B_in["w1"],
        "B_b1": _B_in["b1"],
        "B_w2": _B_in["w2"],
        "B_b2": _B_in["b2"],
    }

def reference(A_x, A_wqkv, A_wo, B_x, B_w1, B_b1, B_w2, B_b2):
    def _A_ref(x, wqkv, wo):
        B, S, D = x.shape
        H = 32; Dh = D // H; W = 256
        qkv = (lambda x, *d: x.permute(*d))((x @ wqkv).reshape(B, S, 3, H, Dh), 2, 0, 3, 1, 4)
        q, k, v = qkv[0], qkv[1], qkv[2]
        scores = torch.einsum("bhqd,bhkd->bhqk", q, k) * (Dh ** -0.5)
        idx = torch.arange(S)
        mask = (idx[:, None] - idx[None, :] >= 0) & (idx[:, None] - idx[None, :] < W)
        scores = torch.where(mask[None, None], scores, -1e9)
        p = _softmax(scores, axis=-1)
        o = torch.einsum("bhqk,bhkd->bhqd", p, v)
        return (lambda x, *d: x.permute(*d))(o, 0, 2, 1, 3).reshape(B, S, D) @ wo

    def _B_ref(x, w1, b1, w2, b2):
        y = _layernorm(x, axis=-1)
        h = _gelu(torch.einsum("bsd,df->bsf", y, w1) + b1)
        return torch.einsum("bsf,fd->bsd", h, w2) + b2

    x = A_x
    h = x + _A_ref(_layernorm(x, axis=-1), A_wqkv, A_wo)
    h2 = h + _B_ref(_layernorm(h, axis=-1), B_w1, B_b1, B_w2, B_b2)
    return h2
```

```cpp
#include <hip/hip_runtime.h>
#include <hip/hip_bf16.h>
#include <stdint.h>

typedef unsigned short bf16_t;
typedef short bf16x8 __attribute__((ext_vector_type(8)));
typedef short s16x4 __attribute__((ext_vector_type(4)));
typedef float f32x4 __attribute__((ext_vector_type(4)));
typedef unsigned u32x2 __attribute__((ext_vector_type(2)));
typedef unsigned u32x4 __attribute__((ext_vector_type(4)));
typedef __bf16 bf2_t __attribute__((ext_vector_type(2)));
#define LAS __attribute__((address_space(3)))

constexpr int S_LEN = 2048, D_MODEL = 1024, D_FF = 3584, N_HEAD = 32, D_HEAD = 32, WIN = 256;
constexpr float LN_EPS = 1e-5f;

__device__ __forceinline__ float bf2f(bf16_t v) { return __uint_as_float(((unsigned)v) << 16); }
__device__ __forceinline__ unsigned pack2(float lo, float hi) {
    bf2_t r; r[0] = (__bf16)lo; r[1] = (__bf16)hi; return __builtin_bit_cast(unsigned, r);
}
__device__ __forceinline__ float rbf(float v) { return bf2f((bf16_t)(pack2(v, 0.f) & 0xffffu)); }
__device__ __forceinline__ float dot2acc(unsigned a, unsigned b, float c) {
    return __builtin_amdgcn_fdot2_f32_bf16(__builtin_bit_cast(bf2_t, a), __builtin_bit_cast(bf2_t, b), c, false);
}
__device__ __forceinline__ float gelu_tanh(float v) {
    const float u = v * (0.7978845608028654f + 0.035677408136300125f * v * v);
    const float e = __builtin_amdgcn_exp2f(u * -2.8853900817779268f);
    return v * __builtin_amdgcn_rcpf(1.0f + e);
}

#define WAIT_VM(n) asm volatile("s_waitcnt vmcnt(%0)" ::"i"(n) : "memory")
#define WAIT_LGKM0() asm volatile("s_waitcnt lgkmcnt(0)" ::: "memory")
#define BAR() __builtin_amdgcn_s_barrier()

enum { EPI_QKV = 0, EPI_WO = 1, EPI_MLP1 = 2, EPI_MLP2 = 3 };

template <int BM, int BN, int WM, int WN, int NS, int EPI, int KDIM, int NDIM, int XM, int XN>
__global__ __launch_bounds__(64 * WM * WN) void gemm_k(const bf16_t* __restrict__ A, const bf16_t* __restrict__ B,
                                                       bf16_t* __restrict__ C, const bf16_t* __restrict__ res,
                                                       const bf16_t* __restrict__ bias) {
    constexpr int NW = WM * WN, TM = BM / WM, TN = BN / WN, FM = TM / 16, FN = TN / 16;
    constexpr int NB = BN / 16;
    constexpr int A_BYTES = BM * 128, B_BYTES = BN * 128, SB = A_BYTES + B_BYTES;
    constexpr int NA = BM / 8, NBI = BN / 8, NLD = NA + NBI;
    constexpr int LPW = (NLD + NW - 1) / NW;
    constexpr int KT = KDIM / 64;
    constexpr bool LN = (EPI == EPI_QKV || EPI == EPI_MLP1);
    static_assert(NLD % NW == 0, "loads must divide");
    extern __shared__ __attribute__((aligned(16))) char smem[];

    const int tid = threadIdx.x, lane = tid & 63;
    const int wid = __builtin_amdgcn_readfirstlane(tid >> 6);
    const int wm = wid / WN, wn = wid % WN;
    const int g = lane >> 4, r16 = lane & 15;

    constexpr int TILES_N = NDIM / BN;
    int tm_i, tn_i;
    {
        const int bid = blockIdx.x, xcd = bid & 7, idx = bid >> 3;
        constexpr int XCOLS = TILES_N / XN;
        const int xr = xcd / XCOLS, xc = xcd % XCOLS;
        tm_i = xr * XM + idx / XN;
        tn_i = xc * XN + idx % XN;
    }
    const int m0 = tm_i * BM, n0 = tn_i * BN;

    const bf16_t* src[LPW];
#pragma unroll
    for (int j = 0; j < LPW; ++j) {
        const int i = wid + j * NW;
        if (i < NA) {
            const int row = 8 * i + (lane >> 3), c = (lane & 7) ^ (lane >> 3);
            src[j] = A + (size_t)(m0 + row) * KDIM + c * 8;
        } else {
            const int s = 4 * (i - NA) + (lane >> 4), kb = s / NB, nb = s % NB, t = lane & 15;
            const int kr = (t >> 1) ^ ((kb & 1) << 2);
            src[j] = B + (size_t)(kb * 8 + kr) * NDIM + n0 + nb * 16 + (t & 1) * 8;
        }
    }
    auto stage = [&](int kt, int buf) {
#pragma unroll
        for (int j = 0; j < LPW; ++j) {
            const int i = wid + j * NW;
            const bf16_t* p = (i < NA) ? src[j] + kt * 64 : src[j] + (size_t)kt * 64 * NDIM;
            __builtin_amdgcn_global_load_lds((const unsigned*)p, (LAS unsigned*)(smem + buf * SB + i * 1024), 16, 0, 0);
        }
    };

    int a_off[FM];
#pragma unroll
    for (int m = 0; m < FM; ++m) {
        const int row = wm * TM + m * 16 + r16;
        a_off[m] = row * 128 + ((g ^ (row & 7)) << 4);
    }
    const int q4 = (lane & 15) >> 2, p4 = lane & 3;
    const int b_off0 = A_BYTES + (g * NB + wn * FN) * 256 + (((g & 1) << 2) + q4) * 32 + p4 * 8;

    f32x4 acc[FM][FN];
#pragma unroll
    for (int m = 0; m < FM; ++m)
#pragma unroll
        for (int n = 0; n < FN; ++n) acc[m][n] = (f32x4){0.f, 0.f, 0.f, 0.f};
    float rs1[FM], rs2[FM], csum[FN];
#pragma unroll
    for (int m = 0; m < FM; ++m) { rs1[m] = 0.f; rs2[m] = 0.f; }
#pragma unroll
    for (int n = 0; n < FN; ++n) csum[n] = 0.f;

    bf16x8 af[2][FM], bfr[2][FN];
    auto load_frags = [&](int set, int buf, int kk) {
        const char* base = smem + buf * SB;
#pragma unroll
        for (int m = 0; m < FM; ++m) af[set][m] = *(const bf16x8*)(base + (a_off[m] ^ (kk << 6)));
#pragma unroll
        for (int n = 0; n < FN; ++n) {
            const int o = b_off0 + kk * (4 * NB * 256) + n * 256;
            s16x4 lo = __builtin_amdgcn_ds_read_tr16_b64_v4i16((LAS s16x4*)(base + o));
            s16x4 hi = __builtin_amdgcn_ds_read_tr16_b64_v4i16((LAS s16x4*)(base + (o ^ 128)));
            bfr[set][n] = (bf16x8){lo[0], lo[1], lo[2], lo[3], hi[0], hi[1], hi[2], hi[3]};
        }
    };
    auto compute = [&](int set) {
#pragma unroll
        for (int m = 0; m < FM; ++m)
#pragma unroll
            for (int n = 0; n < FN; ++n)
                acc[m][n] = __builtin_amdgcn_mfma_f32_16x16x32_bf16(bfr[set][n], af[set][m], acc[m][n], 0, 0, 0);
        if constexpr (LN) {
            const unsigned ones = 0x3f803f80u;
#pragma unroll
            for (int m = 0; m < FM; ++m) {
                const u32x4 w = __builtin_bit_cast(u32x4, af[set][m]);
#pragma unroll
                for (int j = 0; j < 4; ++j) { rs1[m] = dot2acc(w[j], ones, rs1[m]); rs2[m] = dot2acc(w[j], w[j], rs2[m]); }
            }
#pragma unroll
            for (int n = 0; n < FN; ++n) {
                const u32x4 w = __builtin_bit_cast(u32x4, bfr[set][n]);
#pragma unroll
                for (int j = 0; j < 4; ++j) csum[n] = dot2acc(w[j], ones, csum[n]);
            }
        }
    };

#pragma unroll
    for (int s = 0; s < NS - 1; ++s) stage(s, s);
    WAIT_VM(LPW * (NS - 2));
    BAR();
    load_frags(0, 0, 0);
    for (int s = 0; s < KT; ++s) {
        const int buf = s % NS;
        if (s + NS - 1 < KT) stage(s + NS - 1, (s + NS - 1) % NS);
        load_frags(1, buf, 1);
        compute(0);
        if (s + NS - 1 < KT) { WAIT_VM(LPW * (NS - 2)); }
        else { WAIT_VM(0); }
        WAIT_LGKM0();
        BAR();
        if (s + 1 < KT) load_frags(0, (s + 1) % NS, 0);
        compute(1);
    }

    float mean[FM], rstd[FM];
    float cs4[FN][4];
    if constexpr (LN) {
#pragma unroll
        for (int m = 0; m < FM; ++m) {
            float s1 = rs1[m], s2 = rs2[m];
            s1 += __shfl_xor(s1, 16); s1 += __shfl_xor(s1, 32);
            s2 += __shfl_xor(s2, 16); s2 += __shfl_xor(s2, 32);
            const float mu = s1 * (1.0f / KDIM);
            float var = s2 * (1.0f / KDIM) - mu * mu;
            var = fmaxf(var, 0.f);
            float r = __builtin_amdgcn_rsqf(var + LN_EPS);
            if constexpr (EPI == EPI_MLP1) {
                const float v2 = var * r * r;
                r *= __builtin_amdgcn_rsqf(v2 + LN_EPS);
            }
            mean[m] = mu; rstd[m] = r;
        }
#pragma unroll
        for (int n = 0; n < FN; ++n) {
            float c = csum[n];
            c += __shfl_xor(c, 16); c += __shfl_xor(c, 32);
#pragma unroll
            for (int e = 0; e < 4; ++e) cs4[n][e] = __shfl(c, 4 * g + e);
        }
    }
#pragma unroll
    for (int n = 0; n < FN; ++n) {
        const int col = n0 + wn * TN + n * 16 + 4 * g;
        float bv[4] = {0.f, 0.f, 0.f, 0.f};
        if constexpr (EPI == EPI_MLP1 || EPI == EPI_MLP2) {
            const u32x2 b2 = *(const u32x2*)(bias + col);
            bv[0] = bf2f(b2[0] & 0xffff); bv[1] = bf2f(b2[0] >> 16); bv[2] = bf2f(b2[1] & 0xffff); bv[3] = bf2f(b2[1] >> 16);
        }
#pragma unroll
        for (int m = 0; m < FM; ++m) {
            const int row = m0 + wm * TM + m * 16 + r16;
            float v[4];
#pragma unroll
            for (int e = 0; e < 4; ++e) v[e] = acc[m][n][e];
            if constexpr (LN) {
#pragma unroll
                for (int e = 0; e < 4; ++e) v[e] = rstd[m] * (v[e] - mean[m] * cs4[n][e]);
            }
            if constexpr (EPI == EPI_MLP1) {
#pragma unroll
                for (int e = 0; e < 4; ++e) v[e] = gelu_tanh(v[e] + bv[e]);
            }
            if constexpr (EPI == EPI_WO || EPI == EPI_MLP2) {
                const u32x2 rr = *(const u32x2*)(res + (size_t)row * NDIM + col);
                const float r0 = bf2f(rr[0] & 0xffff), r1 = bf2f(rr[0] >> 16), r2 = bf2f(rr[1] & 0xffff), r3 = bf2f(rr[1] >> 16);
                if constexpr (EPI == EPI_MLP2) {
#pragma unroll
                    for (int e = 0; e < 4; ++e) v[e] += bv[e];
                }
                v[0] = r0 + rbf(v[0]); v[1] = r1 + rbf(v[1]); v[2] = r2 + rbf(v[2]); v[3] = r3 + rbf(v[3]);
            }
            u32x2 o; o[0] = pack2(v[0], v[1]); o[1] = pack2(v[2], v[3]);
            *(u32x2*)(C + (size_t)row * NDIM + col) = o;
        }
    }
}

constexpr int AT_ROWS = 320;
__global__ __launch_bounds__(256) void attn_k(const bf16_t* __restrict__ qkv, bf16_t* __restrict__ o) {
    extern __shared__ __attribute__((aligned(16))) char smem[];
    char* Ks = smem;
    char* Vs = smem + AT_ROWS * 64;
    const int tid = threadIdx.x, lane = tid & 63;
    const int wid = __builtin_amdgcn_readfirstlane(tid >> 6);
    const int g = lane >> 4, r16 = lane & 15;
    const int head = blockIdx.x & 31, qt = blockIdx.x >> 5;
    const int q0 = qt * 64, kbase = q0 - 256;
    constexpr int LDQ = 3 * D_MODEL;

#pragma unroll
    for (int j = 0; j < 5; ++j) {
        const int i = wid + 4 * j;
        const int row = 16 * i + (lane >> 2), p = lane & 3;
        int key = kbase + row; key = key < 0 ? 0 : key;
        const int ck = p ^ ((0x78 >> (2 * ((row >> 2) & 3))) & 3);
        const int cv = p ^ (((row >> 3) & 1) << 1);
        const bf16_t* kp = qkv + (size_t)key * LDQ + D_MODEL + head * 32 + ck * 8;
        const bf16_t* vp = qkv + (size_t)key * LDQ + 2 * D_MODEL + head * 32 + cv * 8;
        __builtin_amdgcn_global_load_lds((const unsigned*)kp, (LAS unsigned*)(Ks + i * 1024), 16, 0, 0);
        __builtin_amdgcn_global_load_lds((const unsigned*)vp, (LAS unsigned*)(Vs + i * 1024), 16, 0, 0);
    }
    const int qrow = q0 + wid * 16 + r16;
    const bf16x8 qf = *(const bf16x8*)(qkv + (size_t)qrow * LDQ + head * 32 + g * 8);
    WAIT_VM(0);
    __syncthreads();

    const int rstart = (wid >> 1) * 32;
    f32x4 sc[9][2];
#pragma unroll
    for (int b = 0; b < 9; ++b)
#pragma unroll
        for (int e = 0; e < 2; ++e) {
            const int row = rstart + 32 * b + 8 * (r16 >> 2) + 4 * e + (r16 & 3);
            const int c = g ^ ((0x78 >> (2 * ((row >> 2) & 3))) & 3);
            const bf16x8 kf = *(const bf16x8*)(Ks + row * 64 + c * 16);
            sc[b][e] = __builtin_amdgcn_mfma_f32_16x16x32_bf16(kf, qf, (f32x4){0.f, 0.f, 0.f, 0.f}, 0, 0, 0);
        }
    const int dq = qrow - (kbase + rstart + 8 * g);
    const int kmin = -(kbase + rstart + 8 * g);
    float mx = -3.0e38f;
#pragma unroll
    for (int b = 0; b < 9; ++b)
#pragma unroll
        for (int e = 0; e < 2; ++e)
#pragma unroll
            for (int r = 0; r < 4; ++r) {
                const int off = 32 * b + 4 * e + r;
                const bool ok = ((unsigned)(dq - off) < (unsigned)WIN) && (off >= kmin);
                const float v = ok ? sc[b][e][r] : -3.0e38f;
                sc[b][e][r] = v;
                mx = fmaxf(mx, v);
            }
    mx = fmaxf(mx, __shfl_xor(mx, 16));
    mx = fmaxf(mx, __shfl_xor(mx, 32));
    constexpr float CS = 0.17677669529663687f * 1.4426950408889634f;
    const float mb = mx * CS;
    float sum = 0.f;
    bf16x8 pf[9];
#pragma unroll
    for (int b = 0; b < 9; ++b) {
        float p[8];
#pragma unroll
        for (int e = 0; e < 2; ++e)
#pragma unroll
            for (int r = 0; r < 4; ++r) {
                const float x = __builtin_amdgcn_exp2f(fmaf(sc[b][e][r], CS, -mb));
                p[4 * e + r] = x; sum += x;
            }
        u32x4 w = {pack2(p[0], p[1]), pack2(p[2], p[3]), pack2(p[4], p[5]), pack2(p[6], p[7])};
        pf[b] = __builtin_bit_cast(bf16x8, w);
    }
    sum += __shfl_xor(sum, 16);
    sum += __shfl_xor(sum, 32);
    f32x4 oa[2] = {(f32x4){0.f, 0.f, 0.f, 0.f}, (f32x4){0.f, 0.f, 0.f, 0.f}};
    const int q4 = r16 >> 2, p4 = lane & 3;
#pragma unroll
    for (int b = 0; b < 9; ++b) {
#pragma unroll
        for (int dh = 0; dh < 2; ++dh) {
            s16x4 v2[2];
#pragma unroll
            for (int t = 0; t < 2; ++t) {
                const int row = rstart + 32 * b + 8 * g + 4 * t + q4;
                const int c = (2 * dh + (p4 >> 1)) ^ (((row >> 3) & 1) << 1);
                v2[t] = __builtin_amdgcn_ds_read_tr16_b64_v4i16((LAS s16x4*)(Vs + row * 64 + c * 16 + (p4 & 1) * 8));
            }
            const bf16x8 vf = (bf16x8){v2[0][0], v2[0][1], v2[0][2], v2[0][3], v2[1][0], v2[1][1], v2[1][2], v2[1][3]};
            oa[dh] = __builtin_amdgcn_mfma_f32_16x16x32_bf16(vf, pf[b], oa[dh], 0, 0, 0);
        }
    }
    const float inv = __builtin_amdgcn_rcpf(sum);
#pragma unroll
    for (int dh = 0; dh < 2; ++dh) {
        u32x2 w; w[0] = pack2(oa[dh][0] * inv, oa[dh][1] * inv); w[1] = pack2(oa[dh][2] * inv, oa[dh][3] * inv);
        *(u32x2*)(o + (size_t)qrow * D_MODEL + head * 32 + dh * 16 + 4 * g) = w;
    }
}

template <typename KF>
static void set_lds(KF kf, int bytes) {
    hipFuncSetAttribute((const void*)kf, hipFuncAttributeMaxDynamicSharedMemorySize, bytes);
}

extern "C" void kernel_launch(void* const* d_in, const int* in_sizes, int n_in, void* d_out, int out_size,
                              void* d_ws, size_t ws_size, hipStream_t stream) {
    const bf16_t* x = (const bf16_t*)d_in[0];
    const bf16_t* wqkv = (const bf16_t*)d_in[1];
    const bf16_t* wo = (const bf16_t*)d_in[2];
    const bf16_t* w1 = (const bf16_t*)d_in[4];
    const bf16_t* b1 = (const bf16_t*)d_in[5];
    const bf16_t* w2 = (const bf16_t*)d_in[6];
    const bf16_t* b2 = (const bf16_t*)d_in[7];
    bf16_t* out = (bf16_t*)d_out;
    char* ws = (char*)d_ws;
    bf16_t* qkv = (bf16_t*)ws;
    bf16_t* ob = (bf16_t*)(ws + (size_t)S_LEN * 3 * D_MODEL * 2);
    bf16_t* h = ob + (size_t)S_LEN * D_MODEL;
    bf16_t* h1 = h + (size_t)S_LEN * D_MODEL;

    {
        auto kf = gemm_k<128, 192, 2, 4, 3, EPI_QKV, 1024, 3072, 4, 8>;
        constexpr int lds = 3 * (128 + 192) * 128;
        set_lds(kf, lds);
        kf<<<256, 512, lds, stream>>>(x, wqkv, qkv, nullptr, nullptr);
    }
    {
        constexpr int lds = 2 * AT_ROWS * 64;
        attn_k<<<32 * 32, 256, lds, stream>>>(qkv, ob);
    }
    {
        auto kf = gemm_k<128, 64, 2, 2, 4, EPI_WO, 1024, 1024, 4, 8>;
        constexpr int lds = 4 * (128 + 64) * 128;
        set_lds(kf, lds);
        kf<<<256, 256, lds, stream>>>(ob, wo, h, x, nullptr);
    }
    {
        auto kf = gemm_k<128, 224, 2, 2, 3, EPI_MLP1, 1024, 3584, 8, 4>;
        constexpr int lds = 3 * (128 + 224) * 128;
        set_lds(kf, lds);
        kf<<<256, 256, lds, stream>>>(h, w1, h1, nullptr, b1);
    }
    {
        auto kf = gemm_k<128, 64, 2, 2, 4, EPI_MLP2, 3584, 1024, 4, 8>;
        constexpr int lds = 4 * (128 + 64) * 128;
        set_lds(kf, lds);
        kf<<<256, 256, lds, stream>>>(h1, w2, out, h, b2);
    }
}
```

```cpp
#include <hip/hip_runtime.h>
#include <hip/hip_bf16.h>
#include <stdint.h>
#include <type_traits>

typedef unsigned short bf16_t;
typedef short bf16x8 __attribute__((ext_vector_type(8)));
typedef short s16x4 __attribute__((ext_vector_type(4)));
typedef float f32x4 __attribute__((ext_vector_type(4)));
typedef unsigned u32x2 __attribute__((ext_vector_type(2)));
typedef unsigned u32x4 __attribute__((ext_vector_type(4)));
typedef __bf16 bf2_t __attribute__((ext_vector_type(2)));
#define LAS __attribute__((address_space(3)))

constexpr int S_LEN = 2048, D_MODEL = 1024, D_FF = 3584, WIN = 256;
constexpr float LN_EPS = 1e-5f;

__device__ __forceinline__ float bf2f(unsigned v) { return __uint_as_float(v << 16); }
__device__ __forceinline__ float bfhi(unsigned v) { return __uint_as_float(v & 0xffff0000u); }
__device__ __forceinline__ unsigned pack2(float lo, float hi) {
    bf2_t r; r[0] = (__bf16)lo; r[1] = (__bf16)hi; return __builtin_bit_cast(unsigned, r);
}
__device__ __forceinline__ float gelu_tanh(float v) {
    const float u = v * (0.7978845608028654f + 0.035677408136300125f * v * v);
    const float e = __builtin_amdgcn_exp2f(u * -2.8853900817779268f);
    return v * __builtin_amdgcn_rcpf(1.0f + e);
}

#define WAIT_VM(n) asm volatile("s_waitcnt vmcnt(%0)" ::"i"(n) : "memory")
#define WAIT_LGKM0() asm volatile("s_waitcnt lgkmcnt(0)" ::: "memory")
#define BAR() __builtin_amdgcn_s_barrier()
#define DSR128(dst, addr, off) asm volatile("ds_read_b128 %0, %1 offset:%2" : "=v"(dst) : "v"(addr), "i"(off))
#define DSRTR(dst, addr, off) asm volatile("ds_read_b64_tr_b16 %0, %1 offset:%2" : "=v"(dst) : "v"(addr), "i"(off))
#define SB0() __builtin_amdgcn_sched_barrier(0)

enum { EPI_QKV = 0, EPI_WO = 1, EPI_MLP1 = 2, EPI_MLP2 = 3 };

template <int FM, int FN> struct Frags { bf16x8 a[FM]; s16x4 bl[FN], bh[FN]; };

template <int FM, int FN> __device__ __forceinline__ void wait_frags(Frags<FM, FN>& f) {
    asm volatile("s_waitcnt lgkmcnt(0)" ::: "memory");
#pragma unroll
    for (int m = 0; m < FM; ++m) asm volatile("" : "+v"(f.a[m]));
#pragma unroll
    for (int n = 0; n < FN; ++n) { asm volatile("" : "+v"(f.bl[n])); asm volatile("" : "+v"(f.bh[n])); }
}

template <int BM, int BN, int NS, int EPI, int KDIM, int NDIM, int XM, int XN, int KS>
__global__ __launch_bounds__(512) void gemm_k(const bf16_t* __restrict__ A, const bf16_t* __restrict__ B,
                                              bf16_t* __restrict__ C, const bf16_t* __restrict__ res,
                                              const bf16_t* __restrict__ bias, float* __restrict__ rstat,
                                              const float* __restrict__ csum, float* __restrict__ part,
                                              unsigned* __restrict__ flags) {
#if defined(__HIP_DEVICE_COMPILE__)
    constexpr int WM = 2, WN = 2, NC = 4, NL = 4, NW = 8;
    constexpr int TM = BM / WM, TN = BN / WN, FM = TM / 16, FN = TN / 16;
    constexpr int NB = BN / 16;
    constexpr int A_BYTES = BM * 128, B_BYTES = BN * 128, SB = A_BYTES + B_BYTES;
    constexpr int NA = BM / 8, NBI = BN / 8, NLD = NA + NBI;
    constexpr int LPW = NLD / NL, LPA = NA / NL;
    constexpr int KT = KDIM / 64 / KS;
    constexpr bool LN = (EPI == EPI_QKV || EPI == EPI_MLP1);
    constexpr int NMF = FM * FN, NRD = FM + 2 * FN;
    constexpr int CST = BN * 2 + 16;
    static_assert(NLD % NL == 0 && NA % NL == 0, "loads must divide");
    static_assert(BM * CST <= NS * SB, "epilogue staging must fit");
    extern __shared__ __attribute__((aligned(16))) char smem[];

    const int tid = threadIdx.x, lane = tid & 63;
    const int wid = __builtin_amdgcn_readfirstlane(tid >> 6);
    const int g = lane >> 4, r16 = lane & 15;

    constexpr int TILES_N = NDIM / BN;
    int tm_i, tn_i, kidx = 0;
    {
        const int bid = blockIdx.x, idx = bid >> 3;
        int xcd = bid & 7;
        if constexpr (KS == 2) { kidx = xcd & 1; xcd >>= 1; }
        constexpr int XCOLS = TILES_N / XN;
        const int xr = xcd / XCOLS, xc = xcd % XCOLS;
        tm_i = xr * XM + idx / XN;
        tn_i = xc * XN + idx % XN;
    }
    const int m0 = tm_i * BM, n0 = tn_i * BN;
    const int kb0 = kidx * KT;

    float* p_st = (float*)(smem + NS * SB);
    float* p_cs = p_st + BM * 2;
    float* p_bi = p_cs + BN;

    if (wid >= NC) {
        const __amdgpu_buffer_rsrc_t rsA = __builtin_amdgcn_make_buffer_rsrc((void*)(A + (size_t)m0 * KDIM), 0, BM * KDIM * 2, 0x00020000);
        const __amdgpu_buffer_rsrc_t rsB = __builtin_amdgcn_make_buffer_rsrc((void*)B, 0, KDIM * NDIM * 2, 0x00020000);
        const int lw = wid - NC;
        int voff[LPW];
#pragma unroll
        for (int j = 0; j < LPW; ++j) {
            const int i = lw + j * NL;
            if (j < LPA) {
                const int row = 8 * i + (lane >> 3), c = (lane & 7) ^ (lane >> 3);
                voff[j] = (row * KDIM + c * 8) * 2;
            } else {
                const int s = 4 * (i - NA) + (lane >> 4), kb = s / NB, nb = s % NB, t = lane & 15;
                const int kr = (t >> 1) ^ ((kb & 1) << 2);
                voff[j] = ((kb * 8 + kr) * NDIM + n0 + nb * 16 + (t & 1) * 8) * 2;
            }
        }
        auto dma = [&](int j, int kt, int buf) {
            const int i = lw + j * NL;
            LAS void* dst = (LAS void*)(smem + buf * SB + i * 1024);
            if (j < LPA) __builtin_amdgcn_raw_ptr_buffer_load_lds(rsA, dst, 16, voff[j], (kb0 + kt) * 128, 0, 0);
            else __builtin_amdgcn_raw_ptr_buffer_load_lds(rsB, dst, 16, voff[j], (kb0 + kt) * 128 * NDIM, 0, 0);
        };
#pragma unroll
        for (int s = 0; s < NS - 1; ++s)
#pragma unroll
            for (int j = 0; j < LPW; ++j) dma(j, s, s);
        WAIT_VM(LPW * (NS - 2));
        BAR();
        int dbuf = NS - 1;
        for (int s = 0; s < KT - NS + 1; ++s) {
#pragma unroll
            for (int j = 0; j < LPW; ++j) dma(j, s + NS - 1, dbuf);
            WAIT_VM(LPW * (NS - 2));
            dbuf = (dbuf + 1 == NS) ? 0 : dbuf + 1;
            BAR();
        }
        {
            const int lt = tid - NC * 64;
            if constexpr (EPI == EPI_QKV) {
                p_st[lt] = rstat[(size_t)m0 * 2 + lt];
            }
            if constexpr (EPI == EPI_MLP1) {
                const float* pp = rstat + (size_t)(m0 + (lt >> 1)) * 32 + (lt & 1) * 16;
                const f32x4 u0 = *(const f32x4*)pp, u1 = *(const f32x4*)(pp + 4), u2 = *(const f32x4*)(pp + 8), u3 = *(const f32x4*)(pp + 12);
                float s1 = ((u0[0] + u0[2]) + (u1[0] + u1[2])) + ((u2[0] + u2[2]) + (u3[0] + u3[2]));
                float s2 = ((u0[1] + u0[3]) + (u1[1] + u1[3])) + ((u2[1] + u2[3]) + (u3[1] + u3[3]));
                s1 += __shfl_xor(s1, 1); s2 += __shfl_xor(s2, 1);
                const float mu = s1 * (1.0f / KDIM);
                const float var = fmaxf(s2 * (1.0f / KDIM) - mu * mu, 0.f);
                float r = __builtin_amdgcn_rsqf(var + LN_EPS);
                const float v2 = var * r * r;
                r *= __builtin_amdgcn_rsqf(v2 + LN_EPS);
                p_st[lt] = (lt & 1) ? r : mu;
            }
            if constexpr (LN) { if (lt < BN) p_cs[lt] = csum[n0 + lt]; }
            if constexpr (EPI == EPI_MLP1 || EPI == EPI_MLP2) { if (lt < BN) p_bi[lt] = bf2f(bias[n0 + lt]); }
            WAIT_LGKM0();
        }
        WAIT_VM(0);
#pragma unroll
        for (int s = KT - NS + 1; s < KT - 1; ++s) BAR();
        BAR();
    } else {
        const int wm = wid >> 1, wn = wid & 1;
        const unsigned lds0 = (unsigned)(uintptr_t)(LAS char*)smem;
        const int arow = wm * TM + r16;
        const unsigned a_b0 = lds0 + arow * 128 + ((g ^ (arow & 7)) << 4);
        const unsigned a_b1 = a_b0 ^ 64;
        const int q4 = (lane & 15) >> 2, p4 = lane & 3;
        const unsigned b_b0 = lds0 + A_BYTES + (g * NB + wn * FN) * 256 + (((g & 1) << 2) + q4) * 32 + p4 * 8;
        const unsigned b_b1 = b_b0 ^ 128;

        f32x4 acc[FM][FN];
#pragma unroll
        for (int m = 0; m < FM; ++m)
#pragma unroll
            for (int n = 0; n < FN; ++n) acc[m][n] = (f32x4){0.f, 0.f, 0.f, 0.f};

        Frags<FM, FN> F0, F1;
        auto phase = [&](Frags<FM, FN>& cur, Frags<FM, FN>& nxt, auto kkn_c, auto rd_c, unsigned boff) {
            constexpr int KKN = decltype(kkn_c)::value;
            constexpr bool RD = decltype(rd_c)::value;
            const unsigned aa = (KKN ? a_b1 : a_b0) + boff, bb0 = b_b0 + boff, bb1 = b_b1 + boff;
            constexpr int RPM = (NRD + NMF - 1) / NMF;
#pragma unroll
            for (int i = 0; i < NMF; ++i) {
                const int m = i % FM, n = i / FM;
                const bf16x8 bf = __builtin_shufflevector(cur.bl[n], cur.bh[n], 0, 1, 2, 3, 4, 5, 6, 7);
                acc[m][n] = __builtin_amdgcn_mfma_f32_16x16x32_bf16(bf, cur.a[m], acc[m][n], 0, 0, 0);
                if constexpr (RD) {
#pragma unroll
                    for (int rr = 0; rr < RPM; ++rr) {
                        const int r = i * RPM + rr;
                        if (r < FM) { DSR128(nxt.a[r < FM ? r : 0], aa, (r < FM ? r : 0) * 2048); }
                        else if (r < NRD) {
                            const int nn = (r - FM) >> 1;
                            if ((r - FM) & 1) { DSRTR(nxt.bh[nn < FN ? nn : 0], bb1, KKN * 4 * NB * 256 + (nn < FN ? nn : 0) * 256); }
                            else { DSRTR(nxt.bl[nn < FN ? nn : 0], bb0, KKN * 4 * NB * 256 + (nn < FN ? nn : 0) * 256); }
                        }
                    }
                }
                SB0();
            }
            if constexpr (RD) wait_frags(nxt);
            SB0();
        };
        using T_ = std::integral_constant<bool, true>;
        using F_ = std::integral_constant<bool, false>;
        using K0 = std::integral_constant<int, 0>;
        using K1 = std::integral_constant<int, 1>;

        BAR();
#pragma unroll
        for (int m = 0; m < FM; ++m) DSR128(F0.a[m], a_b0, m * 2048);
#pragma unroll
        for (int n = 0; n < FN; ++n) { DSRTR(F0.bl[n], b_b0, n * 256); DSRTR(F0.bh[n], b_b1, n * 256); }
        wait_frags(F0);
        SB0();
        int buf = 0;
        for (int s = 0; s < KT - 1; ++s) {
            const int nbuf = (buf + 1 == NS) ? 0 : buf + 1;
            phase(F0, F1, K1{}, T_{}, buf * SB);
            BAR();
            SB0();
            phase(F1, F0, K0{}, T_{}, nbuf * SB);
            buf = nbuf;
        }
        phase(F0, F1, K1{}, T_{}, buf * SB);
        phase(F1, F0, K0{}, F_{}, 0);

        if constexpr (KS == 2) {
            const int tile = tm_i * TILES_N + tn_i;
            const __amdgpu_buffer_rsrc_t rsP = __builtin_amdgcn_make_buffer_rsrc((void*)(part + (size_t)tile * BM * BN), 0, BM * BN * 4, 0x00020000);
            const int pbase = (wm * 2 + wn) * NMF * 1024 + lane * 16;
            if (wm != kidx) {
#pragma unroll
                for (int i = 0; i < NMF; ++i)
                    __builtin_amdgcn_raw_buffer_store_b128(__builtin_bit_cast(u32x4, acc[i % FM][i / FM]), rsP, pbase + i * 1024, 0, 16);
                WAIT_VM(0);
            }
            BAR();
            if (tid == 0) __hip_atomic_store(flags + (tile * 2 + kidx) * 32, 1u, __ATOMIC_RELAXED, __HIP_MEMORY_SCOPE_AGENT);
            if (wm == kidx) {
                unsigned* pf = flags + (tile * 2 + (kidx ^ 1)) * 32;
                for (unsigned spin = 0; spin < (1u << 24); ++spin) {
                    if (__hip_atomic_load(pf, __ATOMIC_RELAXED, __HIP_MEMORY_SCOPE_AGENT) != 0u) break;
                    __builtin_amdgcn_s_sleep(1);
                }
                __builtin_amdgcn_fence(__ATOMIC_ACQUIRE, "wavefront");
                u32x4 t[NMF];
#pragma unroll
                for (int i = 0; i < NMF; ++i) t[i] = __builtin_amdgcn_raw_buffer_load_b128(rsP, pbase + i * 1024, 0, 16);
#pragma unroll
                for (int i = 0; i < NMF; ++i) acc[i % FM][i / FM] += __builtin_bit_cast(f32x4, t[i]);
            }
        } else {
            BAR();
        }

        if (KS == 1 || wm == kidx) {
            float mean[FM], rstd[FM];
            if constexpr (LN) {
#pragma unroll
                for (int m = 0; m < FM; ++m) {
                    const float2 st = *(const float2*)(p_st + (wm * TM + m * 16 + r16) * 2);
                    mean[m] = st.x; rstd[m] = st.y;
                }
            }
#pragma unroll
            for (int n = 0; n < FN; ++n) {
                const int col = wn * TN + n * 16 + 4 * g;
                f32x4 bv = {0.f, 0.f, 0.f, 0.f}, c4 = {0.f, 0.f, 0.f, 0.f};
                if constexpr (EPI == EPI_MLP1 || EPI == EPI_MLP2) bv = *(const f32x4*)(p_bi + col);
                if constexpr (LN) c4 = *(const f32x4*)(p_cs + col);
#pragma unroll
                for (int m = 0; m < FM; ++m) {
                    const int row = wm * TM + m * 16 + r16;
                    float v[4];
#pragma unroll
                    for (int e = 0; e < 4; ++e) v[e] = acc[m][n][e];
                    if constexpr (LN) {
#pragma unroll
                        for (int e = 0; e < 4; ++e) v[e] = rstd[m] * (v[e] - mean[m] * c4[e]);
                    }
                    if constexpr (EPI == EPI_MLP1 || EPI == EPI_MLP2) {
#pragma unroll
                        for (int e = 0; e < 4; ++e) v[e] += bv[e];
                    }
                    u32x2 o; o[0] = pack2(v[0], v[1]); o[1] = pack2(v[2], v[3]);
                    *(u32x2*)(smem + row * CST + col * 2) = o;
                }
            }
        }
    }
    __syncthreads();
    constexpr int CPR = BN / 8;
    constexpr int NCH = BM / KS * CPR;
    const int rbase = kidx * (BM / 2);
#pragma unroll
    for (int c0 = 0; c0 < NCH; c0 += NW * 64) {
        const int c = c0 + tid;
        if (NCH % (NW * 64) == 0 || c < NCH) {
            const int row = rbase + c / CPR, ch = c % CPR;
            u32x4 v = *(const u32x4*)(smem + row * CST + ch * 16);
            const size_t go = (size_t)(m0 + row) * NDIM + n0 + ch * 8;
            if constexpr (EPI == EPI_MLP1) {
#pragma unroll
                for (int j = 0; j < 4; ++j) v[j] = pack2(gelu_tanh(bf2f(v[j])), gelu_tanh(bfhi(v[j])));
            }
            if constexpr (EPI == EPI_WO || EPI == EPI_MLP2) {
                const u32x4 rr = *(const u32x4*)(res + go);
#pragma unroll
                for (int j = 0; j < 4; ++j) v[j] = pack2(bf2f(v[j]) + bf2f(rr[j]), bfhi(v[j]) + bfhi(rr[j]));
            }
            *(u32x4*)(C + go) = v;
            if constexpr (EPI == EPI_WO) {
                static_assert(EPI != EPI_WO || CPR == 8, "row partials assume 64-column tiles");
                float s1 = 0.f, s2 = 0.f;
#pragma unroll
                for (int j = 0; j < 4; ++j) {
                    const float lo = bf2f(v[j]), hi = bfhi(v[j]);
                    s1 += lo + hi; s2 = fmaf(lo, lo, s2); s2 = fmaf(hi, hi, s2);
                }
                s1 += __shfl_xor(s1, 1); s1 += __shfl_xor(s1, 2); s1 += __shfl_xor(s1, 4);
                s2 += __shfl_xor(s2, 1); s2 += __shfl_xor(s2, 2); s2 += __shfl_xor(s2, 4);
                if (ch == 0) *(float2*)(rstat + (size_t)(m0 + row) * 32 + tn_i * 2) = make_float2(s1, s2);
            }
        }
    }
#endif
}

__global__ __launch_bounds__(256) void prep_k(const bf16_t* __restrict__ x, const bf16_t* __restrict__ wqkv,
                                              const bf16_t* __restrict__ w1, float* __restrict__ xstat,
                                              float* __restrict__ cs_qkv, float* __restrict__ cs_w1,
                                              unsigned* __restrict__ flags) {
    __shared__ float red[64][33];
    const int tid = threadIdx.x, lane = tid & 63, wid = tid >> 6;
    if (blockIdx.x >= 208) {
        const int row = (blockIdx.x - 208) * 4 + wid;
        const u32x4* p = (const u32x4*)(x + (size_t)row * D_MODEL);
        const u32x4 a = p[lane], b = p[lane + 64];
        float v[16];
#pragma unroll
        for (int j = 0; j < 4; ++j) {
            v[2 * j] = bf2f(a[j]); v[2 * j + 1] = bfhi(a[j]);
            v[8 + 2 * j] = bf2f(b[j]); v[8 + 2 * j + 1] = bfhi(b[j]);
        }
        float s = 0.f;
#pragma unroll
        for (int j = 0; j < 16; ++j) s += v[j];
#pragma unroll
        for (int o = 1; o < 64; o <<= 1) s += __shfl_xor(s, o);
        const float mu = s * (1.0f / D_MODEL);
        float q = 0.f;
#pragma unroll
        for (int j = 0; j < 16; ++j) { const float d = v[j] - mu; q = fmaf(d, d, q); }
#pragma unroll
        for (int o = 1; o < 64; o <<= 1) q += __shfl_xor(q, o);
        if (lane == 0) *(float2*)(xstat + row * 2) = make_float2(mu, __builtin_amdgcn_rsqf(q * (1.0f / D_MODEL) + LN_EPS));
    } else {
        int cb = blockIdx.x;
        if (cb == 0) flags[tid * 32] = 0u;
        const bf16_t* w; float* out; int ld;
        if (cb < 96) { w = wqkv; out = cs_qkv; ld = 3 * D_MODEL; } else { cb -= 96; w = w1; out = cs_w1; ld = D_FF; }
        const int c = tid & 3, kr = tid >> 2;
        const bf16_t* p = w + (size_t)kr * ld + cb * 32 + c * 8;
        float acc[8] = {0.f, 0.f, 0.f, 0.f, 0.f, 0.f, 0.f, 0.f};
        u32x4 t[16];
#pragma unroll
        for (int i = 0; i < 16; ++i) t[i] = *(const u32x4*)(p + (size_t)i * 64 * ld);
#pragma unroll
        for (int i = 0; i < 16; ++i)
#pragma unroll
            for (int j = 0; j < 4; ++j) { acc[2 * j] += bf2f(t[i][j]); acc[2 * j + 1] += bfhi(t[i][j]); }
#pragma unroll
        for (int j = 0; j < 8; ++j) red[kr][c * 8 + j] = acc[j];
        __syncthreads();
        const int col = tid >> 3, part = tid & 7;
        float sum = 0.f;
#pragma unroll
        for (int i = 0; i < 8; ++i) sum += red[part * 8 + i][col];
        sum += __shfl_xor(sum, 1); sum += __shfl_xor(sum, 2); sum += __shfl_xor(sum, 4);
        if (part == 0) out[cb * 32 + col] = sum;
    }
}

constexpr int AT_ROWS = 384;
template <bool GENERIC>
__device__ __forceinline__ void attn_mask(f32x4 (&sc)[9][2], int dq, int kmin) {
#pragma unroll
    for (int b = 0; b < 9; ++b) {
        if (GENERIC || b == 0 || b == 8) {
#pragma unroll
            for (int e = 0; e < 2; ++e)
#pragma unroll
                for (int r = 0; r < 4; ++r) {
                    const int off = 32 * b + 4 * e + r;
                    bool ok = (unsigned)(dq - off) < (unsigned)WIN;
                    if (GENERIC) ok = ok && (off >= kmin);
                    sc[b][e][r] = ok ? sc[b][e][r] : -3.0e38f;
                }
        }
    }
}
__device__ __forceinline__ int kswz(int row) { return ((row >> 1) & 1) | (((row >> 3) & 3) << 1); }
__device__ __forceinline__ int vswz(int row) { return (row & 2) | (((row >> 3) & 1) << 2); }

__global__ __launch_bounds__(1024) void attn_k(const bf16_t* __restrict__ qkv, bf16_t* __restrict__ o) {
    extern __shared__ __attribute__((aligned(16))) char smem[];
    char* Ks = smem;
    char* Vs = smem + AT_ROWS * 128;
    const int tid = threadIdx.x, lane = tid & 63;
    const int wid = __builtin_amdgcn_readfirstlane(tid >> 6);
    const int g = lane >> 4, r16 = lane & 15;
    const int hp = (blockIdx.x & 7) * 2 + ((blockIdx.x >> 3) & 1), qt = blockIdx.x >> 4;
    const int q0 = qt * 128, kbase = q0 - 256;
    const int hh = wid >> 3, wq = wid & 7;
    constexpr int LDQ = 3 * D_MODEL;

    const int qrow = q0 + wq * 16 + r16;
    const bf16x8 qf = *(const bf16x8*)(qkv + (size_t)qrow * LDQ + hp * 64 + hh * 32 + g * 8);
#pragma unroll
    for (int j = 0; j < 3; ++j) {
        const int i = wid + 16 * j;
        const int row = 8 * i + (lane >> 3), p = lane & 7;
        int key = kbase + row; key = key < 0 ? 0 : key;
        const bf16_t* kp = qkv + (size_t)key * LDQ + D_MODEL + hp * 64 + (p ^ kswz(row)) * 8;
        __builtin_amdgcn_global_load_lds((const unsigned*)kp, (LAS unsigned*)(Ks + i * 1024), 16, 0, 0);
    }
#pragma unroll
    for (int j = 0; j < 3; ++j) {
        const int i = wid + 16 * j;
        const int row = 8 * i + (lane >> 3), p = lane & 7;
        int key = kbase + row; key = key < 0 ? 0 : key;
        const bf16_t* vp = qkv + (size_t)key * LDQ + 2 * D_MODEL + hp * 64 + (p ^ vswz(row)) * 8;
        __builtin_amdgcn_global_load_lds((const unsigned*)vp, (LAS unsigned*)(Vs + i * 1024), 16, 0, 0);
    }
    WAIT_VM(3);
    BAR();

    const int rstart = (wq >> 1) * 32;
    f32x4 sc[9][2];
    {
        const unsigned lk = (unsigned)(uintptr_t)(LAS char*)Ks;
        bf16x8 kf[9][2];
#pragma unroll
        for (int e = 0; e < 2; ++e) {
            const int row = rstart + 8 * (r16 >> 2) + 4 * e + (r16 & 3);
            const unsigned ad = lk + row * 128 + (((hh * 4 + g) ^ kswz(row)) << 4);
#pragma unroll
            for (int b = 0; b < 9; ++b) DSR128(kf[b][e], ad, b * 4096);
        }
        asm volatile("s_waitcnt lgkmcnt(0)" ::: "memory");
#pragma unroll
        for (int b = 0; b < 9; ++b)
#pragma unroll
            for (int e = 0; e < 2; ++e) {
                asm volatile("" : "+v"(kf[b][e]));
                sc[b][e] = __builtin_amdgcn_mfma_f32_16x16x32_bf16(kf[b][e], qf, (f32x4){0.f, 0.f, 0.f, 0.f}, 0, 0, 0);
            }
    }
    const int dq = qrow - (kbase + rstart + 8 * g);
    const int kmin = -(kbase + rstart + 8 * g);
    if (qt < 2) attn_mask<true>(sc, dq, kmin); else attn_mask<false>(sc, dq, kmin);
    float mx = -3.0e38f;
#pragma unroll
    for (int b = 0; b < 9; ++b)
#pragma unroll
        for (int e = 0; e < 2; ++e)
            mx = fmaxf(mx, fmaxf(fmaxf(sc[b][e][0], sc[b][e][1]), fmaxf(sc[b][e][2], sc[b][e][3])));
    mx = fmaxf(mx, __shfl_xor(mx, 16));
    mx = fmaxf(mx, __shfl_xor(mx, 32));
    constexpr float CS = 0.17677669529663687f * 1.4426950408889634f;
    const float mb = mx * CS;
    bf16x8 pf[9];
#pragma unroll
    for (int b = 0; b < 9; ++b) {
        float p[8];
#pragma unroll
        for (int e = 0; e < 2; ++e)
#pragma unroll
            for (int r = 0; r < 4; ++r) p[4 * e + r] = __builtin_amdgcn_exp2f(fmaf(sc[b][e][r], CS, -mb));
        u32x4 w = {pack2(p[0], p[1]), pack2(p[2], p[3]), pack2(p[4], p[5]), pack2(p[6], p[7])};
        pf[b] = __builtin_bit_cast(bf16x8, w);
    }
    f32x4 os = {0.f, 0.f, 0.f, 0.f};
    {
        const u32x4 one4 = {0x3f803f80u, 0x3f803f80u, 0x3f803f80u, 0x3f803f80u};
        const bf16x8 ones = __builtin_bit_cast(bf16x8, one4);
#pragma unroll
        for (int b = 0; b < 9; ++b) os = __builtin_amdgcn_mfma_f32_16x16x32_bf16(ones, pf[b], os, 0, 0, 0);
    }
    WAIT_VM(0);
    BAR();
    f32x4 oa[2] = {(f32x4){0.f, 0.f, 0.f, 0.f}, (f32x4){0.f, 0.f, 0.f, 0.f}};
    {
        const int q4 = r16 >> 2, p4 = lane & 3;
        const unsigned lv = (unsigned)(uintptr_t)(LAS char*)Vs;
        const int row = rstart + 8 * g + q4;
        const unsigned vb = lv + row * 128 + (p4 & 1) * 8;
#pragma unroll
        for (int dh = 0; dh < 2; ++dh) {
            const unsigned ad = vb + (((hh * 4 + 2 * dh + (p4 >> 1)) ^ vswz(row)) << 4);
            s16x4 vl[9], vh[9];
#pragma unroll
            for (int b = 0; b < 9; ++b) { DSRTR(vl[b], ad, b * 4096); DSRTR(vh[b], ad, b * 4096 + 512); }
            asm volatile("s_waitcnt lgkmcnt(0)" ::: "memory");
#pragma unroll
            for (int b = 0; b < 9; ++b) {
                asm volatile("" : "+v"(vl[b])); asm volatile("" : "+v"(vh[b]));
                const bf16x8 vf = __builtin_shufflevector(vl[b], vh[b], 0, 1, 2, 3, 4, 5, 6, 7);
                oa[dh] = __builtin_amdgcn_mfma_f32_16x16x32_bf16(vf, pf[b], oa[dh], 0, 0, 0);
            }
        }
    }
    const float inv = __builtin_amdgcn_rcpf(os[0]);
#pragma unroll
    for (int dh = 0; dh < 2; ++dh) {
        u32x2 w; w[0] = pack2(oa[dh][0] * inv, oa[dh][1] * inv); w[1] = pack2(oa[dh][2] * inv, oa[dh][3] * inv);
        *(u32x2*)(o + (size_t)qrow * D_MODEL + hp * 64 + hh * 32 + dh * 16 + 4 * g) = w;
    }
}

template <typename KF>
static void set_lds(KF kf, int bytes) {
    (void)hipFuncSetAttribute((const void*)kf, hipFuncAttributeMaxDynamicSharedMemorySize, bytes);
}

extern "C" void kernel_launch(void* const* d_in, const int* in_sizes, int n_in, void* d_out, int out_size,
                              void* d_ws, size_t ws_size, hipStream_t stream) {
    const bf16_t* x = (const bf16_t*)d_in[0];
    const bf16_t* wqkv = (const bf16_t*)d_in[1];
    const bf16_t* wo = (const bf16_t*)d_in[2];
    const bf16_t* w1 = (const bf16_t*)d_in[4];
    const bf16_t* b1 = (const bf16_t*)d_in[5];
    const bf16_t* w2 = (const bf16_t*)d_in[6];
    const bf16_t* b2 = (const bf16_t*)d_in[7];
    bf16_t* out = (bf16_t*)d_out;
    char* ws = (char*)d_ws;
    bf16_t* qkv = (bf16_t*)ws;
    bf16_t* ob = (bf16_t*)(ws + (size_t)S_LEN * 3 * D_MODEL * 2);
    bf16_t* h = ob + (size_t)S_LEN * D_MODEL;
    bf16_t* h1 = h + (size_t)S_LEN * D_MODEL;
    float* xstat = (float*)(h1 + (size_t)S_LEN * D_FF);
    float* hstat = xstat + S_LEN * 2;
    float* cs_qkv = hstat + S_LEN * 32;
    float* cs_w1 = cs_qkv + 3 * D_MODEL;
    unsigned* flags = (unsigned*)(cs_w1 + D_FF);
    float* part = (float*)(flags + 256 * 32);

    prep_k<<<720, 256, 0, stream>>>(x, wqkv, w1, xstat, cs_qkv, cs_w1, flags);
    {
        auto kf = gemm_k<128, 192, 3, EPI_QKV, 1024, 3072, 8, 4, 1>;
        constexpr int lds = 3 * (128 + 192) * 128 + 4096;
        set_lds(kf, lds);
        kf<<<256, 512, lds, stream>>>(x, wqkv, qkv, nullptr, nullptr, xstat, cs_qkv, nullptr, nullptr);
    }
    {
        constexpr int lds = 2 * AT_ROWS * 128;
        set_lds(attn_k, lds);
        attn_k<<<256, 1024, lds, stream>>>(qkv, ob);
    }
    {
        auto kf = gemm_k<128, 64, 4, EPI_WO, 1024, 1024, 4, 8, 1>;
        constexpr int lds = 4 * (128 + 64) * 128 + 4096;
        set_lds(kf, lds);
        kf<<<256, 512, lds, stream>>>(ob, wo, h, x, nullptr, hstat, nullptr, nullptr, nullptr);
    }
    {
        auto kf = gemm_k<128, 224, 3, EPI_MLP1, 1024, 3584, 8, 4, 1>;
        constexpr int lds = 3 * (128 + 224) * 128 + 4096;
        set_lds(kf, lds);
        kf<<<256, 512, lds, stream>>>(h, w1, h1, nullptr, b1, hstat, cs_w1, nullptr, nullptr);
    }
    {
        auto kf = gemm_k<128, 128, 4, EPI_MLP2, 3584, 1024, 4, 8, 2>;
        constexpr int lds = 4 * (128 + 128) * 128 + 4096;
        set_lds(kf, lds);
        kf<<<256, 512, lds, stream>>>(h1, w2, out, h, b2, nullptr, nullptr, part, flags);
    }
}
```

```cpp
#include <hip/hip_runtime.h>
#include <hip/hip_bf16.h>
#include <stdint.h>
#include <type_traits>

typedef unsigned short bf16_t;
typedef short bf16x8 __attribute__((ext_vector_type(8)));
typedef short s16x4 __attribute__((ext_vector_type(4)));
typedef float f32x4 __attribute__((ext_vector_type(4)));
typedef unsigned u32x2 __attribute__((ext_vector_type(2)));
typedef unsigned u32x4 __attribute__((ext_vector_type(4)));
typedef __bf16 bf2_t __attribute__((ext_vector_type(2)));
#define LAS __attribute__((address_space(3)))

constexpr int S_LEN = 2048, D_MODEL = 1024, D_FF = 3584, WIN = 256;
constexpr float LN_EPS = 1e-5f;

__device__ __forceinline__ float bf2f(unsigned v) { return __uint_as_float(v << 16); }
__device__ __forceinline__ float bfhi(unsigned v) { return __uint_as_float(v & 0xffff0000u); }
__device__ __forceinline__ unsigned pack2(float lo, float hi) {
    bf2_t r; r[0] = (__bf16)lo; r[1] = (__bf16)hi; return __builtin_bit_cast(unsigned, r);
}
__device__ __forceinline__ float gelu_tanh(float v) {
    const float u = v * (0.7978845608028654f + 0.035677408136300125f * v * v);
    const float e = __builtin_amdgcn_exp2f(u * -2.8853900817779268f);
    return v * __builtin_amdgcn_rcpf(1.0f + e);
}

#define WAIT_VM(n) asm volatile("s_waitcnt vmcnt(%0)" ::"i"(n) : "memory")
#define WAIT_LGKM0() asm volatile("s_waitcnt lgkmcnt(0)" ::: "memory")
#define BAR() __builtin_amdgcn_s_barrier()
#define DSR128(dst, addr, off) asm volatile("ds_read_b128 %0, %1 offset:%2" : "=v"(dst) : "v"(addr), "i"(off))
#define DSRTR(dst, addr, off) asm volatile("ds_read_b64_tr_b16 %0, %1 offset:%2" : "=v"(dst) : "v"(addr), "i"(off))
#define SB0() __builtin_amdgcn_sched_barrier(0)

enum { EPI_QKV = 0, EPI_WO = 1, EPI_MLP1 = 2, EPI_MLP2 = 3 };

template <int FM, int FN> struct Frags { bf16x8 a[FM]; s16x4 bl[FN], bh[FN]; };

template <int FM, int FN> __device__ __forceinline__ void wait_frags(Frags<FM, FN>& f) {
    asm volatile("s_waitcnt lgkmcnt(0)" ::: "memory");
#pragma unroll
    for (int m = 0; m < FM; ++m) asm volatile("" : "+v"(f.a[m]));
#pragma unroll
    for (int n = 0; n < FN; ++n) { asm volatile("" : "+v"(f.bl[n])); asm volatile("" : "+v"(f.bh[n])); }
}

template <int BM, int BN, int WM, int WN, int NS, int EPI, int KDIM, int NDIM, int XM, int XN, int KS>
__global__ __launch_bounds__(64 * (WM * WN + 4)) void gemm_k(const bf16_t* __restrict__ A, const bf16_t* __restrict__ B,
                                              bf16_t* __restrict__ C, const bf16_t* __restrict__ res,
                                              const bf16_t* __restrict__ bias, float* __restrict__ rstat,
                                              const float* __restrict__ csum, float* __restrict__ part,
                                              unsigned* __restrict__ flags) {
#if defined(__HIP_DEVICE_COMPILE__)
    constexpr int NC = WM * WN, NL = 4, NW = NC + NL;
    static_assert(KS == 1 || WM == 2, "split-K exchange assumes two row halves");
    constexpr int TM = BM / WM, TN = BN / WN, FM = TM / 16, FN = TN / 16;
    constexpr int NB = BN / 32;
    constexpr int A_BYTES = BM * 128, B_BYTES = BN * 128, SB = A_BYTES + B_BYTES;
    constexpr int NA = BM / 8, NBI = BN / 8, NLD = NA + NBI;
    constexpr int LPW = NLD / NL, LPA = NA / NL;
    constexpr int KT = KDIM / 64 / KS;
    constexpr bool LN = (EPI == EPI_QKV || EPI == EPI_MLP1);
    constexpr int NMF = FM * FN, NRD = FM + 2 * FN;
    constexpr int CST = BN * 2 + 16;
    static_assert(NLD % NL == 0 && NA % NL == 0, "loads must divide");
    static_assert(BM * CST <= NS * SB, "epilogue staging must fit");
    extern __shared__ __attribute__((aligned(16))) char smem[];

    const int tid = threadIdx.x, lane = tid & 63;
    const int wid = __builtin_amdgcn_readfirstlane(tid >> 6);
    const int g = lane >> 4, r16 = lane & 15;

    constexpr int TILES_N = NDIM / BN;
    int tm_i, tn_i, kidx = 0;
    {
        const int bid = blockIdx.x, idx = bid >> 3;
        int xcd = bid & 7;
        if constexpr (KS == 2) { kidx = xcd & 1; xcd >>= 1; }
        constexpr int XCOLS = TILES_N / XN;
        const int xr = xcd / XCOLS, xc = xcd % XCOLS;
        tm_i = xr * XM + idx / XN;
        tn_i = xc * XN + idx % XN;
    }
    const int m0 = tm_i * BM, n0 = tn_i * BN;
    const int kb0 = kidx * KT;

    float* p_st = (float*)(smem + NS * SB);
    float* p_cs = p_st + BM * 2;
    float* p_bi = p_cs + BN;

    if (wid >= NC) {
        const __amdgpu_buffer_rsrc_t rsA = __builtin_amdgcn_make_buffer_rsrc((void*)(A + (size_t)m0 * KDIM), 0, BM * KDIM * 2, 0x00020000);
        const __amdgpu_buffer_rsrc_t rsB = __builtin_amdgcn_make_buffer_rsrc((void*)B, 0, KDIM * NDIM * 2, 0x00020000);
        const int lw = wid - NC;
        int voff[LPW];
#pragma unroll
        for (int j = 0; j < LPW; ++j) {
            const int i = lw + j * NL;
            if (j < LPA) {
                const int row = 8 * i + (lane >> 3), c = (lane & 7) ^ (lane >> 3);
                voff[j] = (row * KDIM + c * 8) * 2;
            } else {
                const int kb = (i - NA) / NB, nb = (i - NA) % NB, r = lane >> 2;
                const int c = (lane & 3) ^ ((r >> 3) << 1);
                voff[j] = ((kb * 16 + r) * NDIM + n0 + nb * 32 + c * 8) * 2;
            }
        }
        auto dma = [&](int j, int kt, int buf) {
            const int i = lw + j * NL;
            LAS void* dst = (LAS void*)(smem + buf * SB + i * 1024);
            if (j < LPA) __builtin_amdgcn_raw_ptr_buffer_load_lds(rsA, dst, 16, voff[j], (kb0 + kt) * 128, 0, 0);
            else __builtin_amdgcn_raw_ptr_buffer_load_lds(rsB, dst, 16, voff[j], (kb0 + kt) * 128 * NDIM, 0, 0);
        };
#pragma unroll
        for (int s = 0; s < NS - 1; ++s)
#pragma unroll
            for (int j = 0; j < LPW; ++j) dma(j, s, s);
        WAIT_VM(LPW * (NS - 2));
        BAR();
        int dbuf = NS - 1;
        for (int s = 0; s < KT - NS + 1; ++s) {
#pragma unroll
            for (int j = 0; j < LPW; ++j) dma(j, s + NS - 1, dbuf);
            WAIT_VM(LPW * (NS - 2));
            dbuf = (dbuf + 1 == NS) ? 0 : dbuf + 1;
            BAR();
        }
        {
            const int lt = tid - NC * 64;
            if constexpr (EPI == EPI_QKV) {
                p_st[lt] = rstat[(size_t)m0 * 2 + lt];
            }
            if constexpr (EPI == EPI_MLP1) {
                const float* pp = rstat + (size_t)(m0 + (lt >> 1)) * 32 + (lt & 1) * 16;
                const f32x4 u0 = *(const f32x4*)pp, u1 = *(const f32x4*)(pp + 4), u2 = *(const f32x4*)(pp + 8), u3 = *(const f32x4*)(pp + 12);
                float s1 = ((u0[0] + u0[2]) + (u1[0] + u1[2])) + ((u2[0] + u2[2]) + (u3[0] + u3[2]));
                float s2 = ((u0[1] + u0[3]) + (u1[1] + u1[3])) + ((u2[1] + u2[3]) + (u3[1] + u3[3]));
                s1 += __shfl_xor(s1, 1); s2 += __shfl_xor(s2, 1);
                const float mu = s1 * (1.0f / KDIM);
                const float var = fmaxf(s2 * (1.0f / KDIM) - mu * mu, 0.f);
                float r = __builtin_amdgcn_rsqf(var + LN_EPS);
                const float v2 = var * r * r;
                r *= __builtin_amdgcn_rsqf(v2 + LN_EPS);
                p_st[lt] = (lt & 1) ? r : mu;
            }
            if constexpr (LN) { if (lt < BN) p_cs[lt] = csum[n0 + lt]; }
            if constexpr (EPI == EPI_MLP1 || EPI == EPI_MLP2) { if (lt < BN) p_bi[lt] = bf2f(bias[n0 + lt]); }
            WAIT_LGKM0();
        }
        WAIT_VM(0);
#pragma unroll
        for (int s = KT - NS + 1; s < KT - 1; ++s) BAR();
        BAR();
    } else {
        const int wm = wid / WN, wn = wid % WN;
        const unsigned lds0 = (unsigned)(uintptr_t)(LAS char*)smem;
        const int arow = wm * TM + r16;
        const unsigned a_b0 = lds0 + arow * 128 + ((g ^ (arow & 7)) << 4);
        const unsigned a_b1 = a_b0 ^ 64;
        const int q4 = (lane & 15) >> 2, p4 = lane & 3;
        const int ce = (wn * FN) & 1;
        const unsigned b_w0 = lds0 + A_BYTES + ((g >> 1) * NB + ((wn * FN) >> 1)) * 1024 + (8 * (g & 1) + q4) * 64 + (p4 & 1) * 8;
        const unsigned b_b0 = b_w0 + (((2 * ce + (p4 >> 1)) ^ ((g & 1) << 1)) << 4);
        const unsigned b_b1 = b_w0 + ce * 1024 + (((2 * (1 - ce) + (p4 >> 1)) ^ ((g & 1) << 1)) << 4);

        f32x4 acc[FM][FN];
#pragma unroll
        for (int m = 0; m < FM; ++m)
#pragma unroll
            for (int n = 0; n < FN; ++n) acc[m][n] = (f32x4){0.f, 0.f, 0.f, 0.f};

        Frags<FM, FN> F0, F1;
        auto phase = [&](Frags<FM, FN>& cur, Frags<FM, FN>& nxt, auto kkn_c, auto rd_c, unsigned boff) {
            constexpr int KKN = decltype(kkn_c)::value;
            constexpr bool RD = decltype(rd_c)::value;
            const unsigned aa = (KKN ? a_b1 : a_b0) + boff, bb0 = b_b0 + boff, bb1 = b_b1 + boff;
            constexpr int RPM = (NRD + NMF - 1) / NMF;
#pragma unroll
            for (int i = 0; i < NMF; ++i) {
                const int m = i % FM, n = i / FM;
                const bf16x8 bf = __builtin_shufflevector(cur.bl[n], cur.bh[n], 0, 1, 2, 3, 4, 5, 6, 7);
                acc[m][n] = __builtin_amdgcn_mfma_f32_16x16x32_bf16(bf, cur.a[m], acc[m][n], 0, 0, 0);
                if constexpr (RD) {
#pragma unroll
                    for (int rr = 0; rr < RPM; ++rr) {
                        const int r = i * RPM + rr;
                        if (r < FM) { DSR128(nxt.a[r < FM ? r : 0], aa, (r < FM ? r : 0) * 2048); }
                        else if (r < NRD) {
                            const int nn = ((r - FM) >> 1) < FN ? (r - FM) >> 1 : 0;
                            if ((r - FM) & 1) { DSRTR(nxt.bh[nn], (nn & 1) ? bb1 : bb0, KKN * 2 * NB * 1024 + (nn >> 1) * 1024 + 256); }
                            else { DSRTR(nxt.bl[nn], (nn & 1) ? bb1 : bb0, KKN * 2 * NB * 1024 + (nn >> 1) * 1024); }
                        }
                    }
                }
                SB0();
            }
            if constexpr (RD) wait_frags(nxt);
            SB0();
        };
        using T_ = std::integral_constant<bool, true>;
        using F_ = std::integral_constant<bool, false>;
        using K0 = std::integral_constant<int, 0>;
        using K1 = std::integral_constant<int, 1>;

        BAR();
#pragma unroll
        for (int m = 0; m < FM; ++m) DSR128(F0.a[m], a_b0, m * 2048);
#pragma unroll
        for (int n = 0; n < FN; ++n) { DSRTR(F0.bl[n], (n & 1) ? b_b1 : b_b0, (n >> 1) * 1024); DSRTR(F0.bh[n], (n & 1) ? b_b1 : b_b0, (n >> 1) * 1024 + 256); }
        wait_frags(F0);
        SB0();
        int buf = 0;
        for (int s = 0; s < KT - 1; ++s) {
            const int nbuf = (buf + 1 == NS) ? 0 : buf + 1;
            phase(F0, F1, K1{}, T_{}, buf * SB);
            BAR();
            SB0();
            phase(F1, F0, K0{}, T_{}, nbuf * SB);
            buf = nbuf;
        }
        phase(F0, F1, K1{}, T_{}, buf * SB);
        phase(F1, F0, K0{}, F_{}, 0);

        if constexpr (KS == 2) {
            const int tile = tm_i * TILES_N + tn_i;
            const __amdgpu_buffer_rsrc_t rsP = __builtin_amdgcn_make_buffer_rsrc((void*)(part + (size_t)tile * BM * BN), 0, BM * BN * 4, 0x00020000);
            const int pbase = (wm * WN + wn) * NMF * 1024 + lane * 16;
            if (wm != kidx) {
#pragma unroll
                for (int i = 0; i < NMF; ++i)
                    __builtin_amdgcn_raw_buffer_store_b128(__builtin_bit_cast(u32x4, acc[i % FM][i / FM]), rsP, pbase + i * 1024, 0, 16);
                WAIT_VM(0);
            }
            BAR();
            if (tid == 0) __hip_atomic_store(flags + (tile * 2 + kidx) * 32, 1u, __ATOMIC_RELAXED, __HIP_MEMORY_SCOPE_AGENT);
            if (wm == kidx) {
                unsigned* pf = flags + (tile * 2 + (kidx ^ 1)) * 32;
                for (unsigned spin = 0; spin < (1u << 24); ++spin) {
                    if (__hip_atomic_load(pf, __ATOMIC_RELAXED, __HIP_MEMORY_SCOPE_AGENT) != 0u) break;
                    __builtin_amdgcn_s_sleep(1);
                }
                __builtin_amdgcn_fence(__ATOMIC_ACQUIRE, "wavefront");
                u32x4 t[NMF];
#pragma unroll
                for (int i = 0; i < NMF; ++i) t[i] = __builtin_amdgcn_raw_buffer_load_b128(rsP, pbase + i * 1024, 0, 16);
#pragma unroll
                for (int i = 0; i < NMF; ++i) acc[i % FM][i / FM] += __builtin_bit_cast(f32x4, t[i]);
            }
        } else {
            BAR();
        }

        if (KS == 1 || wm == kidx) {
            float mean[FM], rstd[FM];
            if constexpr (LN) {
#pragma unroll
                for (int m = 0; m < FM; ++m) {
                    const float2 st = *(const float2*)(p_st + (wm * TM + m * 16 + r16) * 2);
                    mean[m] = st.x; rstd[m] = st.y;
                }
            }
#pragma unroll
            for (int n = 0; n < FN; ++n) {
                const int col = wn * TN + n * 16 + 4 * g;
                f32x4 bv = {0.f, 0.f, 0.f, 0.f}, c4 = {0.f, 0.f, 0.f, 0.f};
                if constexpr (EPI == EPI_MLP1 || EPI == EPI_MLP2) bv = *(const f32x4*)(p_bi + col);
                if constexpr (LN) c4 = *(const f32x4*)(p_cs + col);
#pragma unroll
                for (int m = 0; m < FM; ++m) {
                    const int row = wm * TM + m * 16 + r16;
                    float v[4];
#pragma unroll
                    for (int e = 0; e < 4; ++e) v[e] = acc[m][n][e];
                    if constexpr (LN) {
#pragma unroll
                        for (int e = 0; e < 4; ++e) v[e] = rstd[m] * (v[e] - mean[m] * c4[e]);
                    }
                    if constexpr (EPI == EPI_MLP1 || EPI == EPI_MLP2) {
#pragma unroll
                        for (int e = 0; e < 4; ++e) v[e] += bv[e];
                    }
                    u32x2 o; o[0] = pack2(v[0], v[1]); o[1] = pack2(v[2], v[3]);
                    *(u32x2*)(smem + row * CST + col * 2) = o;
                }
            }
        }
    }
    __syncthreads();
    constexpr int CPR = BN / 8;
    constexpr int NCH = BM / KS * CPR;
    const int rbase = kidx * (BM / 2);
#pragma unroll
    for (int c0 = 0; c0 < NCH; c0 += NW * 64) {
        const int c = c0 + tid;
        if (NCH % (NW * 64) == 0 || c < NCH) {
            const int row = rbase + c / CPR, ch = c % CPR;
            u32x4 v = *(const u32x4*)(smem + row * CST + ch * 16);
            const size_t go = (size_t)(m0 + row) * NDIM + n0 + ch * 8;
            if constexpr (EPI == EPI_MLP1) {
#pragma unroll
                for (int j = 0; j < 4; ++j) v[j] = pack2(gelu_tanh(bf2f(v[j])), gelu_tanh(bfhi(v[j])));
            }
            if constexpr (EPI == EPI_WO || EPI == EPI_MLP2) {
                const u32x4 rr = *(const u32x4*)(res + go);
#pragma unroll
                for (int j = 0; j < 4; ++j) v[j] = pack2(bf2f(v[j]) + bf2f(rr[j]), bfhi(v[j]) + bfhi(rr[j]));
            }
            *(u32x4*)(C + go) = v;
            if constexpr (EPI == EPI_WO) {
                static_assert(EPI != EPI_WO || CPR == 8, "row partials assume 64-column tiles");
                float s1 = 0.f, s2 = 0.f;
#pragma unroll
                for (int j = 0; j < 4; ++j) {
                    const float lo = bf2f(v[j]), hi = bfhi(v[j]);
                    s1 += lo + hi; s2 = fmaf(lo, lo, s2); s2 = fmaf(hi, hi, s2);
                }
                s1 += __shfl_xor(s1, 1); s1 += __shfl_xor(s1, 2); s1 += __shfl_xor(s1, 4);
                s2 += __shfl_xor(s2, 1); s2 += __shfl_xor(s2, 2); s2 += __shfl_xor(s2, 4);
                if (ch == 0) *(float2*)(rstat + (size_t)(m0 + row) * 32 + tn_i * 2) = make_float2(s1, s2);
            }
        }
    }
#endif
}

__global__ __launch_bounds__(256) void prep_k(const bf16_t* __restrict__ x, const bf16_t* __restrict__ wqkv,
                                              const bf16_t* __restrict__ w1, float* __restrict__ xstat,
                                              float* __restrict__ cs_qkv, float* __restrict__ cs_w1,
                                              unsigned* __restrict__ flags, const u32x4* __restrict__ pf0, const u32x4* __restrict__ pf1) {
    __shared__ float red[64][33];
    const int tid = threadIdx.x, lane = tid & 63, wid = tid >> 6;
    if (blockIdx.x >= 720) {
        const int b = blockIdx.x - 720;
        const u32x4* p = b < 32 ? pf0 + (size_t)b * 4096 : pf1 + (size_t)(b - 32) * 4096;
        u32x4 t[16];
#pragma unroll
        for (int i = 0; i < 16; ++i) t[i] = p[i * 256 + tid];
        unsigned acc = 0;
#pragma unroll
        for (int i = 0; i < 16; ++i) acc ^= t[i][0] ^ t[i][3];
        asm volatile("" :: "v"(acc));
        return;
    }
    if (blockIdx.x >= 208) {
        const int row = (blockIdx.x - 208) * 4 + wid;
        const u32x4* p = (const u32x4*)(x + (size_t)row * D_MODEL);
        const u32x4 a = p[lane], b = p[lane + 64];
        float v[16];
#pragma unroll
        for (int j = 0; j < 4; ++j) {
            v[2 * j] = bf2f(a[j]); v[2 * j + 1] = bfhi(a[j]);
            v[8 + 2 * j] = bf2f(b[j]); v[8 + 2 * j + 1] = bfhi(b[j]);
        }
        float s = 0.f;
#pragma unroll
        for (int j = 0; j < 16; ++j) s += v[j];
#pragma unroll
        for (int o = 1; o < 64; o <<= 1) s += __shfl_xor(s, o);
        const float mu = s * (1.0f / D_MODEL);
        float q = 0.f;
#pragma unroll
        for (int j = 0; j < 16; ++j) { const float d = v[j] - mu; q = fmaf(d, d, q); }
#pragma unroll
        for (int o = 1; o < 64; o <<= 1) q += __shfl_xor(q, o);
        if (lane == 0) *(float2*)(xstat + row * 2) = make_float2(mu, __builtin_amdgcn_rsqf(q * (1.0f / D_MODEL) + LN_EPS));
    } else {
        int cb = blockIdx.x;
        if (cb == 0) flags[tid * 32] = 0u;
        const bf16_t* w; float* out; int ld;
        if (cb < 96) { w = wqkv; out = cs_qkv; ld = 3 * D_MODEL; } else { cb -= 96; w = w1; out = cs_w1; ld = D_FF; }
        const int c = tid & 3, kr = tid >> 2;
        const bf16_t* p = w + (size_t)kr * ld + cb * 32 + c * 8;
        float acc[8] = {0.f, 0.f, 0.f, 0.f, 0.f, 0.f, 0.f, 0.f};
        u32x4 t[16];
#pragma unroll
        for (int i = 0; i < 16; ++i) t[i] = *(const u32x4*)(p + (size_t)i * 64 * ld);
#pragma unroll
        for (int i = 0; i < 16; ++i)
#pragma unroll
            for (int j = 0; j < 4; ++j) { acc[2 * j] += bf2f(t[i][j]); acc[2 * j + 1] += bfhi(t[i][j]); }
#pragma unroll
        for (int j = 0; j < 8; ++j) red[kr][c * 8 + j] = acc[j];
        __syncthreads();
        const int col = tid >> 3, part = tid & 7;
        float sum = 0.f;
#pragma unroll
        for (int i = 0; i < 8; ++i) sum += red[part * 8 + i][col];
        sum += __shfl_xor(sum, 1); sum += __shfl_xor(sum, 2); sum += __shfl_xor(sum, 4);
        if (part == 0) out[cb * 32 + col] = sum;
    }
}

constexpr int AT_ROWS = 384;
template <bool GENERIC>
__device__ __forceinline__ void attn_mask(f32x4 (&sc)[9][2], int dq, int kmin) {
#pragma unroll
    for (int b = 0; b < 9; ++b) {
        if (GENERIC || b == 0 || b == 8) {
#pragma unroll
            for (int e = 0; e < 2; ++e)
#pragma unroll
                for (int r = 0; r < 4; ++r) {
                    const int off = 32 * b + 4 * e + r;
                    bool ok = (unsigned)(dq - off) < (unsigned)WIN;
                    if (GENERIC) ok = ok && (off >= kmin);
                    sc[b][e][r] = ok ? sc[b][e][r] : -3.0e38f;
                }
        }
    }
}
__device__ __forceinline__ int kswz(int row) { return ((row >> 1) & 1) | (((row >> 3) & 3) << 1); }
__device__ __forceinline__ int vswz(int row) { return (row & 2) | (((row >> 3) & 1) << 2); }

__global__ __launch_bounds__(1024) void attn_k(const bf16_t* __restrict__ qkv, bf16_t* __restrict__ o) {
    extern __shared__ __attribute__((aligned(16))) char smem[];
    char* Ks = smem;
    char* Vs = smem + AT_ROWS * 128;
    const int tid = threadIdx.x, lane = tid & 63;
    const int wid = __builtin_amdgcn_readfirstlane(tid >> 6);
    const int g = lane >> 4, r16 = lane & 15;
    const int hp = (blockIdx.x & 7) * 2 + ((blockIdx.x >> 3) & 1), qt = blockIdx.x >> 4;
    const int q0 = qt * 128, kbase = q0 - 256;
    const int hh = wid >> 3, wq = wid & 7;
    constexpr int LDQ = 3 * D_MODEL;

    const int qrow = q0 + wq * 16 + r16;
    const bf16x8 qf = *(const bf16x8*)(qkv + (size_t)qrow * LDQ + hp * 64 + hh * 32 + g * 8);
#pragma unroll
    for (int j = 0; j < 3; ++j) {
        const int i = wid + 16 * j;
        const int row = 8 * i + (lane >> 3), p = lane & 7;
        int key = kbase + row; key = key < 0 ? 0 : key;
        const bf16_t* kp = qkv + (size_t)key * LDQ + D_MODEL + hp * 64 + (p ^ kswz(row)) * 8;
        __builtin_amdgcn_global_load_lds((const unsigned*)kp, (LAS unsigned*)(Ks + i * 1024), 16, 0, 0);
    }
#pragma unroll
    for (int j = 0; j < 3; ++j) {
        const int i = wid + 16 * j;
        const int row = 8 * i + (lane >> 3), p = lane & 7;
        int key = kbase + row; key = key < 0 ? 0 : key;
        const bf16_t* vp = qkv + (size_t)key * LDQ + 2 * D_MODEL + hp * 64 + (p ^ vswz(row)) * 8;
        __builtin_amdgcn_global_load_lds((const unsigned*)vp, (LAS unsigned*)(Vs + i * 1024), 16, 0, 0);
    }
    WAIT_VM(3);
    BAR();

    const int rstart = (wq >> 1) * 32;
    f32x4 sc[9][2];
    {
        const unsigned lk = (unsigned)(uintptr_t)(LAS char*)Ks;
        bf16x8 kf[9][2];
#pragma unroll
        for (int e = 0; e < 2; ++e) {
            const int row = rstart + 8 * (r16 >> 2) + 4 * e + (r16 & 3);
            const unsigned ad = lk + row * 128 + (((hh * 4 + g) ^ kswz(row)) << 4);
#pragma unroll
            for (int b = 0; b < 9; ++b) DSR128(kf[b][e], ad, b * 4096);
        }
        asm volatile("s_waitcnt lgkmcnt(0)" ::: "memory");
#pragma unroll
        for (int b = 0; b < 9; ++b)
#pragma unroll
            for (int e = 0; e < 2; ++e) {
                asm volatile("" : "+v"(kf[b][e]));
                sc[b][e] = __builtin_amdgcn_mfma_f32_16x16x32_bf16(kf[b][e], qf, (f32x4){0.f, 0.f, 0.f, 0.f}, 0, 0, 0);
            }
    }
    const int dq = qrow - (kbase + rstart + 8 * g);
    const int kmin = -(kbase + rstart + 8 * g);
    if (qt < 2) attn_mask<true>(sc, dq, kmin); else attn_mask<false>(sc, dq, kmin);
    float mx = -3.0e38f;
#pragma unroll
    for (int b = 0; b < 9; ++b)
#pragma unroll
        for (int e = 0; e < 2; ++e)
            mx = fmaxf(mx, fmaxf(fmaxf(sc[b][e][0], sc[b][e][1]), fmaxf(sc[b][e][2], sc[b][e][3])));
    mx = fmaxf(mx, __shfl_xor(mx, 16));
    mx = fmaxf(mx, __shfl_xor(mx, 32));
    constexpr float CS = 0.17677669529663687f * 1.4426950408889634f;
    const float mb = mx * CS;
    bf16x8 pf[9];
#pragma unroll
    for (int b = 0; b < 9; ++b) {
        float p[8];
#pragma unroll
        for (int e = 0; e < 2; ++e)
#pragma unroll
            for (int r = 0; r < 4; ++r) p[4 * e + r] = __builtin_amdgcn_exp2f(fmaf(sc[b][e][r], CS, -mb));
        u32x4 w = {pack2(p[0], p[1]), pack2(p[2], p[3]), pack2(p[4], p[5]), pack2(p[6], p[7])};
        pf[b] = __builtin_bit_cast(bf16x8, w);
    }
    f32x4 os = {0.f, 0.f, 0.f, 0.f};
    {
        const u32x4 one4 = {0x3f803f80u, 0x3f803f80u, 0x3f803f80u, 0x3f803f80u};
        const bf16x8 ones = __builtin_bit_cast(bf16x8, one4);
#pragma unroll
        for (int b = 0; b < 9; ++b) os = __builtin_amdgcn_mfma_f32_16x16x32_bf16(ones, pf[b], os, 0, 0, 0);
    }
    WAIT_VM(0);
    BAR();
    f32x4 oa[2] = {(f32x4){0.f, 0.f, 0.f, 0.f}, (f32x4){0.f, 0.f, 0.f, 0.f}};
    {
        const int q4 = r16 >> 2, p4 = lane & 3;
        const unsigned lv = (unsigned)(uintptr_t)(LAS char*)Vs;
        const int row = rstart + 8 * g + q4;
        const unsigned vb = lv + row * 128 + (p4 & 1) * 8;
#pragma unroll
        for (int dh = 0; dh < 2; ++dh) {
            const unsigned ad = vb + (((hh * 4 + 2 * dh + (p4 >> 1)) ^ vswz(row)) << 4);
            s16x4 vl[9], vh[9];
#pragma unroll
            for (int b = 0; b < 9; ++b) { DSRTR(vl[b], ad, b * 4096); DSRTR(vh[b], ad, b * 4096 + 512); }
            asm volatile("s_waitcnt lgkmcnt(0)" ::: "memory");
#pragma unroll
            for (int b = 0; b < 9; ++b) {
                asm volatile("" : "+v"(vl[b])); asm volatile("" : "+v"(vh[b]));
                const bf16x8 vf = __builtin_shufflevector(vl[b], vh[b], 0, 1, 2, 3, 4, 5, 6, 7);
                oa[dh] = __builtin_amdgcn_mfma_f32_16x16x32_bf16(vf, pf[b], oa[dh], 0, 0, 0);
            }
        }
    }
    const float inv = __builtin_amdgcn_rcpf(os[0]);
#pragma unroll
    for (int dh = 0; dh < 2; ++dh) {
        u32x2 w; w[0] = pack2(oa[dh][0] * inv, oa[dh][1] * inv); w[1] = pack2(oa[dh][2] * inv, oa[dh][3] * inv);
        *(u32x2*)(o + (size_t)qrow * D_MODEL + hp * 64 + hh * 32 + dh * 16 + 4 * g) = w;
    }
}

#define QKV_WM 2
#define QKV_WN 4
#define WO_WM 4
#define WO_WN 2
#define M1_WM 4
#define M1_WN 2
#define M2_WM 2
#define M2_WN 4
template <typename KF>
static void set_lds(KF kf, int bytes) {
    (void)hipFuncSetAttribute((const void*)kf, hipFuncAttributeMaxDynamicSharedMemorySize, bytes);
}

extern "C" void kernel_launch(void* const* d_in, const int* in_sizes, int n_in, void* d_out, int out_size,
                              void* d_ws, size_t ws_size, hipStream_t stream) {
    const bf16_t* x = (const bf16_t*)d_in[0];
    const bf16_t* wqkv = (const bf16_t*)d_in[1];
    const bf16_t* wo = (const bf16_t*)d_in[2];
    const bf16_t* w1 = (const bf16_t*)d_in[4];
    const bf16_t* b1 = (const bf16_t*)d_in[5];
    const bf16_t* w2 = (const bf16_t*)d_in[6];
    const bf16_t* b2 = (const bf16_t*)d_in[7];
    bf16_t* out = (bf16_t*)d_out;
    char* ws = (char*)d_ws;
    bf16_t* qkv = (bf16_t*)ws;
    bf16_t* ob = (bf16_t*)(ws + (size_t)S_LEN * 3 * D_MODEL * 2);
    bf16_t* h = ob + (size_t)S_LEN * D_MODEL;
    bf16_t* h1 = h + (size_t)S_LEN * D_MODEL;
    float* xstat = (float*)(h1 + (size_t)S_LEN * D_FF);
    float* hstat = xstat + S_LEN * 2;
    float* cs_qkv = hstat + S_LEN * 32;
    float* cs_w1 = cs_qkv + 3 * D_MODEL;
    unsigned* flags = (unsigned*)(cs_w1 + D_FF);
    float* part = (float*)(flags + 256 * 32);

    prep_k<<<720 + 144, 256, 0, stream>>>(x, wqkv, w1, xstat, cs_qkv, cs_w1, flags, (const u32x4*)wo, (const u32x4*)w2);
    {
        auto kf = gemm_k<128, 192, QKV_WM, QKV_WN, 3, EPI_QKV, 1024, 3072, 8, 4, 1>;
        constexpr int lds = 3 * (128 + 192) * 128 + 4096;
        set_lds(kf, lds);
        kf<<<256, 64 * (QKV_WM * QKV_WN + 4), lds, stream>>>(x, wqkv, qkv, nullptr, nullptr, xstat, cs_qkv, nullptr, nullptr);
    }
    {
        constexpr int lds = 2 * AT_ROWS * 128;
        set_lds(attn_k, lds);
        attn_k<<<256, 1024, lds, stream>>>(qkv, ob);
    }
    {
        auto kf = gemm_k<128, 64, WO_WM, WO_WN, 4, EPI_WO, 1024, 1024, 4, 8, 1>;
        constexpr int lds = 4 * (128 + 64) * 128 + 4096;
        set_lds(kf, lds);
        kf<<<256, 64 * (WO_WM * WO_WN + 4), lds, stream>>>(ob, wo, h, x, nullptr, hstat, nullptr, nullptr, nullptr);
    }
    {
        auto kf = gemm_k<128, 224, M1_WM, M1_WN, 3, EPI_MLP1, 1024, 3584, 8, 4, 1>;
        constexpr int lds = 3 * (128 + 224) * 128 + 4096;
        set_lds(kf, lds);
        kf<<<256, 64 * (M1_WM * M1_WN + 4), lds, stream>>>(h, w1, h1, nullptr, b1, hstat, cs_w1, nullptr, nullptr);
    }
    {
        auto kf = gemm_k<128, 128, M2_WM, M2_WN, 4, EPI_MLP2, 3584, 1024, 4, 8, 2>;
        constexpr int lds = 4 * (128 + 128) * 128 + 4096;
        set_lds(kf, lds);
        kf<<<256, 64 * (M2_WM * M2_WN + 4), lds, stream>>>(h1, w2, out, h, b2, nullptr, nullptr, part, flags);
    }
}
```

```cpp
#include <hip/hip_runtime.h>
#include <hip/hip_bf16.h>
#include <stdint.h>
#include <type_traits>

typedef unsigned short bf16_t;
typedef short bf16x8 __attribute__((ext_vector_type(8)));
typedef short s16x4 __attribute__((ext_vector_type(4)));
typedef float f32x4 __attribute__((ext_vector_type(4)));
typedef unsigned u32x2 __attribute__((ext_vector_type(2)));
typedef unsigned u32x4 __attribute__((ext_vector_type(4)));
typedef __bf16 bf2_t __attribute__((ext_vector_type(2)));
#define LAS __attribute__((address_space(3)))

constexpr int S_LEN = 2048, D_MODEL = 1024, D_FF = 3584, WIN = 256;
constexpr float LN_EPS = 1e-5f;

__device__ __forceinline__ float bf2f(unsigned v) { return __uint_as_float(v << 16); }
__device__ __forceinline__ float bfhi(unsigned v) { return __uint_as_float(v & 0xffff0000u); }
__device__ __forceinline__ unsigned pack2(float lo, float hi) {
    bf2_t r; r[0] = (__bf16)lo; r[1] = (__bf16)hi; return __builtin_bit_cast(unsigned, r);
}
__device__ __forceinline__ float gelu_tanh(float v) {
    const float u = v * (0.7978845608028654f + 0.035677408136300125f * v * v);
    const float e = __builtin_amdgcn_exp2f(u * -2.8853900817779268f);
    return v * __builtin_amdgcn_rcpf(1.0f + e);
}

#define WAIT_VM(n) asm volatile("s_waitcnt vmcnt(%0)" ::"i"(n) : "memory")
#define WAIT_LGKM0() asm volatile("s_waitcnt lgkmcnt(0)" ::: "memory")
#define BAR() __builtin_amdgcn_s_barrier()
#define DSR128(dst, addr, off) asm volatile("ds_read_b128 %0, %1 offset:%2" : "=v"(dst) : "v"(addr), "i"(off))
#define DSRTR(dst, addr, off) asm volatile("ds_read_b64_tr_b16 %0, %1 offset:%2" : "=v"(dst) : "v"(addr), "i"(off))
#define SB0() __builtin_amdgcn_sched_barrier(0)

enum { EPI_QKV = 0, EPI_WO = 1, EPI_MLP1 = 2, EPI_MLP2 = 3 };

template <int FM, int FN> struct Frags { bf16x8 a[FM]; s16x4 bl[FN], bh[FN]; };

template <int FM, int FN> __device__ __forceinline__ void wait_frags(Frags<FM, FN>& f) {
    asm volatile("s_waitcnt lgkmcnt(0)" ::: "memory");
#pragma unroll
    for (int m = 0; m < FM; ++m) asm volatile("" : "+v"(f.a[m]));
#pragma unroll
    for (int n = 0; n < FN; ++n) { asm volatile("" : "+v"(f.bl[n])); asm volatile("" : "+v"(f.bh[n])); }
}

template <int BM, int BN, int WM, int WN, int NS, int EPI, int KDIM, int NDIM, int XM, int XN, int KS>
__global__ __launch_bounds__(64 * (WM * WN + 4)) void gemm_k(const bf16_t* __restrict__ A, const bf16_t* __restrict__ B,
                                              bf16_t* __restrict__ C, const bf16_t* __restrict__ res,
                                              const bf16_t* __restrict__ bias, float* __restrict__ rstat,
                                              const float* __restrict__ csum, float* __restrict__ part,
                                              unsigned* __restrict__ flags) {
#if defined(__HIP_DEVICE_COMPILE__)
    constexpr int NC = WM * WN, NL = 4, NW = NC + NL;
    static_assert(KS == 1 || WM == 2, "split-K exchange assumes two row halves");
    constexpr int TM = BM / WM, TN = BN / WN, FM = TM / 16, FN = TN / 16;
    constexpr int NB = (BN + 63) / 64;
    constexpr int A_BYTES = BM * 128, B_BYTES = NB * 8192, SB = A_BYTES + B_BYTES;
    constexpr int NA = BM / 8, NBI = NB * 8, NLD = NA + NBI;
    constexpr int LPW = NLD / NL, LPA = NA / NL;
    constexpr int KT = KDIM / 64 / KS;
    constexpr bool LN = (EPI == EPI_QKV || EPI == EPI_MLP1);
    constexpr int NMF = FM * FN, NRD = FM + 2 * FN;
    constexpr int CST = BN * 2 + 16;
    static_assert(NLD % NL == 0 && NA % NL == 0, "loads must divide");
    static_assert(BM * CST <= NS * SB, "epilogue staging must fit");
    extern __shared__ __attribute__((aligned(16))) char smem[];

    const int tid = threadIdx.x, lane = tid & 63;
    const int wid = __builtin_amdgcn_readfirstlane(tid >> 6);
    const int g = lane >> 4, r16 = lane & 15;

    constexpr int TILES_N = NDIM / BN;
    int tm_i, tn_i, kidx = 0;
    {
        const int bid = blockIdx.x, idx = bid >> 3;
        int xcd = bid & 7;
        if constexpr (KS == 2) { kidx = xcd & 1; xcd >>= 1; }
        constexpr int XCOLS = TILES_N / XN;
        const int xr = xcd / XCOLS, xc = xcd % XCOLS;
        tm_i = xr * XM + idx / XN;
        tn_i = xc * XN + idx % XN;
    }
    const int m0 = tm_i * BM, n0 = tn_i * BN;
    const int kb0 = kidx * KT;

    constexpr int CPR = BN / 8;
    constexpr int NCH = BM / KS * CPR;
    constexpr int NIT = (NCH + NW * 64 - 1) / (NW * 64);
    const int rbase = kidx * (BM / 2);
    u32x4 rres[NIT];
    if constexpr (EPI == EPI_WO || EPI == EPI_MLP2) {
        static_assert(!(EPI == EPI_WO || EPI == EPI_MLP2) || NCH % (NW * 64) == 0, "even split");
#pragma unroll
        for (int it = 0; it < NIT; ++it) {
            const int c = it * NW * 64 + tid;
            rres[it] = *(const u32x4*)(res + (size_t)(m0 + rbase + c / CPR) * NDIM + n0 + (c % CPR) * 8);
        }
    }

    float* p_st = (float*)(smem + NS * SB);
    float* p_cs = p_st + BM * 2;
    float* p_bi = p_cs + BN;

    if (wid >= NC) {
        const __amdgpu_buffer_rsrc_t rsA = __builtin_amdgcn_make_buffer_rsrc((void*)(A + (size_t)m0 * KDIM), 0, BM * KDIM * 2, 0x00020000);
        const __amdgpu_buffer_rsrc_t rsB = __builtin_amdgcn_make_buffer_rsrc((void*)B, 0, KDIM * NDIM * 2, 0x00020000);
        const int lw = wid - NC;
        int voff[LPW];
#pragma unroll
        for (int j = 0; j < LPW; ++j) {
            const int i = lw + j * NL;
            if (j < LPA) {
                const int row = 8 * i + (lane >> 3), c = (lane & 7) ^ (lane >> 3);
                voff[j] = (row * KDIM + c * 8) * 2;
            } else {
                const int kb = (i - NA) / NB, nb = (i - NA) % NB, r = lane >> 3;
                int c = (lane & 7) ^ ((r & 2) | ((kb & 1) << 2));
                if (nb * 64 + c * 8 >= BN) c &= 3;
                voff[j] = ((kb * 8 + r) * NDIM + n0 + nb * 64 + c * 8) * 2;
            }
        }
        auto dma = [&](int j, int kt, int buf) {
            const int i = lw + j * NL;
            LAS void* dst = (LAS void*)(smem + buf * SB + i * 1024);
            if (j < LPA) __builtin_amdgcn_raw_ptr_buffer_load_lds(rsA, dst, 16, voff[j], (kb0 + kt) * 128, 0, 0);
            else __builtin_amdgcn_raw_ptr_buffer_load_lds(rsB, dst, 16, voff[j], (kb0 + kt) * 128 * NDIM, 0, 0);
        };
#pragma unroll
        for (int s = 0; s < NS - 1; ++s)
#pragma unroll
            for (int j = 0; j < LPW; ++j) dma(j, s, s);
        WAIT_VM(LPW * (NS - 2));
        BAR();
        int dbuf = NS - 1;
#pragma nounroll
        for (int s = 0; s < KT - NS + 1; ++s) {
#pragma unroll
            for (int j = 0; j < LPW; ++j) dma(j, s + NS - 1, dbuf);
            WAIT_VM(LPW * (NS - 2));
            dbuf = (dbuf + 1 == NS) ? 0 : dbuf + 1;
            BAR();
        }
        {
            const int lt = tid - NC * 64;
            if constexpr (EPI == EPI_QKV) {
                p_st[lt] = rstat[(size_t)m0 * 2 + lt];
            }
            if constexpr (EPI == EPI_MLP1) {
                const float* pp = rstat + (size_t)(m0 + (lt >> 1)) * 32 + (lt & 1) * 16;
                const f32x4 u0 = *(const f32x4*)pp, u1 = *(const f32x4*)(pp + 4), u2 = *(const f32x4*)(pp + 8), u3 = *(const f32x4*)(pp + 12);
                float s1 = ((u0[0] + u0[2]) + (u1[0] + u1[2])) + ((u2[0] + u2[2]) + (u3[0] + u3[2]));
                float s2 = ((u0[1] + u0[3]) + (u1[1] + u1[3])) + ((u2[1] + u2[3]) + (u3[1] + u3[3]));
                s1 += __shfl_xor(s1, 1); s2 += __shfl_xor(s2, 1);
                const float mu = s1 * (1.0f / KDIM);
                const float var = fmaxf(s2 * (1.0f / KDIM) - mu * mu, 0.f);
                float r = __builtin_amdgcn_rsqf(var + LN_EPS);
                const float v2 = var * r * r;
                r *= __builtin_amdgcn_rsqf(v2 + LN_EPS);
                p_st[lt] = (lt & 1) ? r : mu;
            }
            if constexpr (LN) { if (lt < BN) p_cs[lt] = (csum[n0 + lt] + csum[NDIM + n0 + lt]) + (csum[2 * NDIM + n0 + lt] + csum[3 * NDIM + n0 + lt]); }
            if constexpr (EPI == EPI_MLP1 || EPI == EPI_MLP2) { if (lt < BN) p_bi[lt] = bf2f(bias[n0 + lt]); }
            WAIT_LGKM0();
        }
        WAIT_VM(0);
#pragma unroll
        for (int s = KT - NS + 1; s < KT - 1; ++s) BAR();
        BAR();
    } else {
        const int wm = wid / WN, wn = wid % WN;
        __builtin_amdgcn_s_setprio(2);
        const unsigned lds0 = (unsigned)(uintptr_t)(LAS char*)smem;
        const int arow = wm * TM + r16;
        const unsigned a_b0 = lds0 + arow * 128 + ((g ^ (arow & 7)) << 4);
        const unsigned a_b1 = a_b0 ^ 64;
        const int q4 = (lane & 15) >> 2, p4 = lane & 3;
        unsigned b_b[4];
#pragma unroll
        for (int j = 0; j < 4; ++j) {
            const int c = wn * FN + j;
            b_b[j] = lds0 + A_BYTES + (g * NB + (c >> 2)) * 1024 + q4 * 128 + (p4 & 1) * 8 +
                     (((2 * (c & 3) + (p4 >> 1)) ^ ((q4 & 2) | ((g & 1) << 2))) << 4);
        }

        f32x4 acc[FM][FN];
#pragma unroll
        for (int m = 0; m < FM; ++m)
#pragma unroll
            for (int n = 0; n < FN; ++n) acc[m][n] = (f32x4){0.f, 0.f, 0.f, 0.f};

        constexpr int PFU = (EPI == EPI_QKV) ? 2 : (EPI == EPI_MLP1 ? 7 : 0);
        u32x4 pf_dummy = {0u, 0u, 0u, 0u};
        const char* pf_ptr = (const char*)part + (size_t)(blockIdx.x * 4 + wid) * PFU * 1024 + lane * 16;
        auto touch = [&](int s) {
            if constexpr (PFU > 0) {
                const int u = (s >> 1) < PFU ? (s >> 1) : PFU - 1;
                const char* a = pf_ptr + u * 1024;
                asm volatile("global_load_dwordx4 %0, %1, off" : "+v"(pf_dummy) : "v"(a));
            }
        };
        Frags<FM, FN> F0, F1;
        auto phase = [&](Frags<FM, FN>& cur, Frags<FM, FN>& nxt, auto kkn_c, auto rd_c, unsigned boff) {
            constexpr int KKN = decltype(kkn_c)::value;
            constexpr bool RD = decltype(rd_c)::value;
            const unsigned aa = (KKN ? a_b1 : a_b0) + boff;
            unsigned bb[4];
#pragma unroll
            for (int j = 0; j < (FN < 4 ? FN : 4); ++j) bb[j] = b_b[j] + boff;
            constexpr int RPM = (NRD + NMF - 1) / NMF;
#pragma unroll
            for (int i = 0; i < NMF; ++i) {
                const int m = i % FM, n = i / FM;
                const bf16x8 bf = __builtin_shufflevector(cur.bl[n], cur.bh[n], 0, 1, 2, 3, 4, 5, 6, 7);
                acc[m][n] = __builtin_amdgcn_mfma_f32_16x16x32_bf16(bf, cur.a[m], acc[m][n], 0, 0, 0);
                if constexpr (RD) {
#pragma unroll
                    for (int rr = 0; rr < RPM; ++rr) {
                        const int r = i * RPM + rr;
                        if (r < FM) { DSR128(nxt.a[r < FM ? r : 0], aa, (r < FM ? r : 0) * 2048); }
                        else if (r < NRD) {
                            const int nn = ((r - FM) >> 1) < FN ? (r - FM) >> 1 : 0;
                            if ((r - FM) & 1) { DSRTR(nxt.bh[nn], bb[nn & 3], KKN * 4 * NB * 1024 + (nn >> 2) * 1024 + 512); }
                            else { DSRTR(nxt.bl[nn], bb[nn & 3], KKN * 4 * NB * 1024 + (nn >> 2) * 1024); }
                        }
                    }
                }
                SB0();
            }
            if constexpr (RD) wait_frags(nxt);
            SB0();
        };
        using T_ = std::integral_constant<bool, true>;
        using F_ = std::integral_constant<bool, false>;
        using K0 = std::integral_constant<int, 0>;
        using K1 = std::integral_constant<int, 1>;

        BAR();
#pragma unroll
        for (int m = 0; m < FM; ++m) DSR128(F0.a[m], a_b0, m * 2048);
#pragma unroll
        for (int n = 0; n < FN; ++n) { DSRTR(F0.bl[n], b_b[n & 3], (n >> 2) * 1024); DSRTR(F0.bh[n], b_b[n & 3], (n >> 2) * 1024 + 512); }
        wait_frags(F0);
        SB0();
        int buf = 0;
#pragma nounroll
        for (int s = 0; s < KT - 1; ++s) {
            const int nbuf = (buf + 1 == NS) ? 0 : buf + 1;
            touch(s);
            SB0();
            phase(F0, F1, K1{}, T_{}, buf * SB);
            BAR();
            SB0();
            phase(F1, F0, K0{}, T_{}, nbuf * SB);
            buf = nbuf;
        }
        phase(F0, F1, K1{}, T_{}, buf * SB);
        phase(F1, F0, K0{}, F_{}, 0);
        if constexpr (PFU > 0) asm volatile("s_waitcnt vmcnt(0)" : "+v"(pf_dummy) :: "memory");

        if constexpr (KS == 2) {
            const int tile = tm_i * TILES_N + tn_i;
            const __amdgpu_buffer_rsrc_t rsP = __builtin_amdgcn_make_buffer_rsrc((void*)(part + (size_t)tile * BM * BN), 0, BM * BN * 4, 0x00020000);
            const int pbase = (wm * WN + wn) * NMF * 1024 + lane * 16;
            if (wm != kidx) {
#pragma unroll
                for (int i = 0; i < NMF; ++i)
                    __builtin_amdgcn_raw_buffer_store_b128(__builtin_bit_cast(u32x4, acc[i % FM][i / FM]), rsP, pbase + i * 1024, 0, 16);
                WAIT_VM(0);
            }
            BAR();
            if (tid == 0) __hip_atomic_store(flags + (tile * 2 + kidx) * 32, 1u, __ATOMIC_RELAXED, __HIP_MEMORY_SCOPE_AGENT);
            if (wm == kidx) {
                unsigned* pf = flags + (tile * 2 + (kidx ^ 1)) * 32;
                for (unsigned spin = 0; spin < (1u << 24); ++spin) {
                    if (__hip_atomic_load(pf, __ATOMIC_RELAXED, __HIP_MEMORY_SCOPE_AGENT) != 0u) break;
                    __builtin_amdgcn_s_sleep(1);
                }
                __builtin_amdgcn_fence(__ATOMIC_ACQUIRE, "wavefront");
                u32x4 t[NMF];
#pragma unroll
                for (int i = 0; i < NMF; ++i) t[i] = __builtin_amdgcn_raw_buffer_load_b128(rsP, pbase + i * 1024, 0, 16);
#pragma unroll
                for (int i = 0; i < NMF; ++i) acc[i % FM][i / FM] += __builtin_bit_cast(f32x4, t[i]);
            }
        } else {
            BAR();
        }

        if (KS == 1 || wm == kidx) {
            float mean[FM], rstd[FM];
            if constexpr (LN) {
#pragma unroll
                for (int m = 0; m < FM; ++m) {
                    const float2 st = *(const float2*)(p_st + (wm * TM + m * 16 + r16) * 2);
                    mean[m] = st.x; rstd[m] = st.y;
                }
            }
#pragma unroll
            for (int n = 0; n < FN; ++n) {
                const int col = wn * TN + n * 16 + 4 * g;
                f32x4 bv = {0.f, 0.f, 0.f, 0.f}, c4 = {0.f, 0.f, 0.f, 0.f};
                if constexpr (EPI == EPI_MLP1 || EPI == EPI_MLP2) bv = *(const f32x4*)(p_bi + col);
                if constexpr (LN) c4 = *(const f32x4*)(p_cs + col);
#pragma unroll
                for (int m = 0; m < FM; ++m) {
                    const int row = wm * TM + m * 16 + r16;
                    float v[4];
#pragma unroll
                    for (int e = 0; e < 4; ++e) v[e] = acc[m][n][e];
                    if constexpr (LN) {
#pragma unroll
                        for (int e = 0; e < 4; ++e) v[e] = rstd[m] * (v[e] - mean[m] * c4[e]);
                    }
                    if constexpr (EPI == EPI_MLP1 || EPI == EPI_MLP2) {
#pragma unroll
                        for (int e = 0; e < 4; ++e) v[e] += bv[e];
                    }
                    u32x2 o; o[0] = pack2(v[0], v[1]); o[1] = pack2(v[2], v[3]);
                    *(u32x2*)(smem + row * CST + col * 2) = o;
                }
            }
        }
    }
    __syncthreads();
    const __amdgpu_buffer_rsrc_t rsC = __builtin_amdgcn_make_buffer_rsrc((void*)(C + (size_t)m0 * NDIM), 0, BM * NDIM * 2, 0x00020000);
#pragma unroll
    for (int it = 0; it < NIT; ++it) {
        const int c = it * NW * 64 + tid;
        if (NCH % (NW * 64) == 0 || c < NCH) {
            const int row = rbase + c / CPR, ch = c % CPR;
            u32x4 v = *(const u32x4*)(smem + row * CST + ch * 16);
            if constexpr (EPI == EPI_MLP1) {
#pragma unroll
                for (int j = 0; j < 4; ++j) v[j] = pack2(gelu_tanh(bf2f(v[j])), gelu_tanh(bfhi(v[j])));
            }
            if constexpr (EPI == EPI_WO || EPI == EPI_MLP2) {
                const u32x4 rr = rres[it];
#pragma unroll
                for (int j = 0; j < 4; ++j) v[j] = pack2(bf2f(v[j]) + bf2f(rr[j]), bfhi(v[j]) + bfhi(rr[j]));
            }
            __builtin_amdgcn_raw_buffer_store_b128(v, rsC, (row * NDIM + n0 + ch * 8) * 2, 0, 16);
            if constexpr (EPI == EPI_WO) {
                static_assert(EPI != EPI_WO || CPR == 8, "row partials assume 64-column tiles");
                float s1 = 0.f, s2 = 0.f;
#pragma unroll
                for (int j = 0; j < 4; ++j) {
                    const float lo = bf2f(v[j]), hi = bfhi(v[j]);
                    s1 += lo + hi; s2 = fmaf(lo, lo, s2); s2 = fmaf(hi, hi, s2);
                }
                s1 += __shfl_xor(s1, 1); s1 += __shfl_xor(s1, 2); s1 += __shfl_xor(s1, 4);
                s2 += __shfl_xor(s2, 1); s2 += __shfl_xor(s2, 2); s2 += __shfl_xor(s2, 4);
                if (ch == 0) *(float2*)(rstat + (size_t)(m0 + row) * 32 + tn_i * 2) = make_float2(s1, s2);
            }
        }
    }
#endif
}

__global__ __launch_bounds__(256) void prep_k(const bf16_t* __restrict__ x, const bf16_t* __restrict__ wqkv,
                                              const bf16_t* __restrict__ w1, float* __restrict__ xstat,
                                              float* __restrict__ cs_qkv, float* __restrict__ cs_w1,
                                              unsigned* __restrict__ flags) {
    __shared__ float red[64][33];
    const int tid = threadIdx.x, lane = tid & 63, wid = tid >> 6;
    if (blockIdx.x >= 208) {
        const int row = (blockIdx.x - 208) * 4 + wid;
        const u32x4* p = (const u32x4*)(x + (size_t)row * D_MODEL);
        const u32x4 a = p[lane], b = p[lane + 64];
        float v[16];
#pragma unroll
        for (int j = 0; j < 4; ++j) {
            v[2 * j] = bf2f(a[j]); v[2 * j + 1] = bfhi(a[j]);
            v[8 + 2 * j] = bf2f(b[j]); v[8 + 2 * j + 1] = bfhi(b[j]);
        }
        float s = 0.f;
#pragma unroll
        for (int j = 0; j < 16; ++j) s += v[j];
#pragma unroll
        for (int o = 1; o < 64; o <<= 1) s += __shfl_xor(s, o);
        const float mu = s * (1.0f / D_MODEL);
        float q = 0.f;
#pragma unroll
        for (int j = 0; j < 16; ++j) { const float d = v[j] - mu; q = fmaf(d, d, q); }
#pragma unroll
        for (int o = 1; o < 64; o <<= 1) q += __shfl_xor(q, o);
        if (lane == 0) *(float2*)(xstat + row * 2) = make_float2(mu, __builtin_amdgcn_rsqf(q * (1.0f / D_MODEL) + LN_EPS));
    } else {
        int cb = blockIdx.x;
        if (cb == 0) flags[tid * 32] = 0u;
        const bf16_t* w; float* out; int ld;
        if (cb < 96) { w = wqkv; out = cs_qkv; ld = 3 * D_MODEL; } else { cb -= 96; w = w1; out = cs_w1; ld = D_FF; }
        const int kq = cb & 3, cblk = cb >> 2;
        const int c = tid & 15, kr = tid >> 4;
        const bf16_t* p = w + (size_t)(kq * 256 + kr) * ld + cblk * 128 + c * 8;
        float acc[8] = {0.f, 0.f, 0.f, 0.f, 0.f, 0.f, 0.f, 0.f};
        u32x4 t[16];
#pragma unroll
        for (int i = 0; i < 16; ++i) t[i] = *(const u32x4*)(p + (size_t)i * 16 * ld);
#pragma unroll
        for (int i = 0; i < 16; ++i)
#pragma unroll
            for (int j = 0; j < 4; ++j) { acc[2 * j] += bf2f(t[i][j]); acc[2 * j + 1] += bfhi(t[i][j]); }
        float* rd = &red[0][0];
#pragma unroll
        for (int j = 0; j < 8; ++j) rd[kr * 132 + c * 8 + j] = acc[j];
        __syncthreads();
        if (tid < 128) {
            float sum = 0.f;
#pragma unroll
            for (int i = 0; i < 16; ++i) sum += rd[i * 132 + tid];
            out[kq * ld + cblk * 128 + tid] = sum;
        }
    }
}

constexpr int AT_ROWS = 384;
template <bool GENERIC>
__device__ __forceinline__ void attn_mask(f32x4 (&sc)[9][2], int dq, unsigned wq) {
#pragma unroll
    for (int b = 0; b < 9; ++b) {
        if (GENERIC || b == 0 || b == 8) {
#pragma unroll
            for (int e = 0; e < 2; ++e)
#pragma unroll
                for (int r = 0; r < 4; ++r) {
                    const int off = 32 * b + 4 * e + r;
                    sc[b][e][r] = ((unsigned)(dq - off) < wq) ? sc[b][e][r] : -3.0e38f;
                }
        }
    }
}
__device__ __forceinline__ int kswz(int row) { return ((row >> 1) & 1) | (((row >> 3) & 3) << 1); }
__device__ __forceinline__ int vswz(int row) { return (row & 2) | (((row >> 3) & 1) << 2); }

__global__ __launch_bounds__(1024) void attn_k(const bf16_t* __restrict__ qkv, bf16_t* __restrict__ o) {
#if defined(__HIP_DEVICE_COMPILE__)
    extern __shared__ __attribute__((aligned(16))) char smem[];
    char* Ks = smem;
    char* Vs = smem + AT_ROWS * 128;
    const int tid = threadIdx.x, lane = tid & 63;
    const int wid = __builtin_amdgcn_readfirstlane(tid >> 6);
    const int g = lane >> 4, r16 = lane & 15;
    const int hp = (blockIdx.x & 7) * 2 + ((blockIdx.x >> 3) & 1), qt = blockIdx.x >> 4;
    const int q0 = qt * 128, kbase = q0 - 256;
    const int hh = wid >> 3, wq = wid & 7;
    constexpr int LDQ = 3 * D_MODEL;

    const int qrow = q0 + wq * 16 + r16;
    bf16x8 qf;
    {
        const bf16_t* qp = qkv + (size_t)qrow * LDQ + hp * 64 + hh * 32 + g * 8;
        asm volatile("global_load_dwordx4 %0, %1, off" : "=v"(qf) : "v"(qp));
    }
#pragma unroll
    for (int j = 0; j < 3; ++j) {
        const int i = wid + 16 * j;
        const int row = 8 * i + (lane >> 3), p = lane & 7;
        int key = kbase + row; key = key < 0 ? 0 : key;
        const bf16_t* kp = qkv + (size_t)key * LDQ + D_MODEL + hp * 64 + (p ^ kswz(row)) * 8;
        __builtin_amdgcn_global_load_lds((const unsigned*)kp, (LAS unsigned*)(Ks + i * 1024), 16, 0, 0);
    }
    BAR();
#pragma unroll
    for (int j = 0; j < 3; ++j) {
        const int i = wid + 16 * j;
        const int row = 8 * i + (lane >> 3), p = lane & 7;
        int key = kbase + row; key = key < 0 ? 0 : key;
        const bf16_t* vp = qkv + (size_t)key * LDQ + 2 * D_MODEL + hp * 64 + (p ^ vswz(row)) * 8;
        __builtin_amdgcn_global_load_lds((const unsigned*)vp, (LAS unsigned*)(Vs + i * 1024), 16, 0, 0);
    }
    asm volatile("s_waitcnt vmcnt(3)" : "+v"(qf) :: "memory");
    BAR();

    const int rstart = (wq >> 1) * 32;
    f32x4 sc[9][2];
    {
        const unsigned lk = (unsigned)(uintptr_t)(LAS char*)Ks;
        bf16x8 kf[9][2];
#pragma unroll
        for (int e = 0; e < 2; ++e) {
            const int row = rstart + 8 * (r16 >> 2) + 4 * e + (r16 & 3);
            const unsigned ad = lk + row * 128 + (((hh * 4 + g) ^ kswz(row)) << 4);
#pragma unroll
            for (int b = 0; b < 9; ++b) DSR128(kf[b][e], ad, b * 4096);
        }
        asm volatile("s_waitcnt lgkmcnt(0)" ::: "memory");
#pragma unroll
        for (int b = 0; b < 9; ++b)
#pragma unroll
            for (int e = 0; e < 2; ++e) {
                asm volatile("" : "+v"(kf[b][e]));
                sc[b][e] = __builtin_amdgcn_mfma_f32_16x16x32_bf16(kf[b][e], qf, (f32x4){0.f, 0.f, 0.f, 0.f}, 0, 0, 0);
            }
    }
    SB0();
    WAIT_VM(0);
    BAR();
    SB0();
    const int dq = qrow - (kbase + rstart + 8 * g);
    const unsigned wq_ = (unsigned)min(WIN, qrow + 1);
    if (qt < 2) attn_mask<true>(sc, dq, wq_); else attn_mask<false>(sc, dq, wq_);
    float mx0 = -3.0e38f, mx1 = -3.0e38f;
#pragma unroll
    for (int b = 0; b < 9; ++b)
#pragma unroll
        for (int e = 0; e < 2; ++e) {
            asm("v_max3_f32 %0, %1, %2, %0" : "+v"(mx0) : "v"(sc[b][e][0]), "v"(sc[b][e][1]));
            asm("v_max3_f32 %0, %1, %2, %0" : "+v"(mx1) : "v"(sc[b][e][2]), "v"(sc[b][e][3]));
        }
    float mx = fmaxf(mx0, mx1);
    mx = fmaxf(mx, __shfl_xor(mx, 16));
    mx = fmaxf(mx, __shfl_xor(mx, 32));
    constexpr float CS = 0.17677669529663687f * 1.4426950408889634f;
    const float mb = mx * CS;
    bf16x8 pf[9];
#pragma unroll
    for (int b = 0; b < 9; ++b) {
        float p[8];
#pragma unroll
        for (int e = 0; e < 2; ++e) {
            const f32x4 t = sc[b][e] * CS - mb;
#pragma unroll
            for (int r = 0; r < 4; ++r) p[4 * e + r] = __builtin_amdgcn_exp2f(t[r]);
        }
        u32x4 w = {pack2(p[0], p[1]), pack2(p[2], p[3]), pack2(p[4], p[5]), pack2(p[6], p[7])};
        pf[b] = __builtin_bit_cast(bf16x8, w);
    }
    f32x4 os = {0.f, 0.f, 0.f, 0.f};
    {
        const u32x4 one4 = {0x3f803f80u, 0x3f803f80u, 0x3f803f80u, 0x3f803f80u};
        const bf16x8 ones = __builtin_bit_cast(bf16x8, one4);
#pragma unroll
        for (int b = 0; b < 9; ++b) os = __builtin_amdgcn_mfma_f32_16x16x32_bf16(ones, pf[b], os, 0, 0, 0);
    }
    f32x4 oa[2] = {(f32x4){0.f, 0.f, 0.f, 0.f}, (f32x4){0.f, 0.f, 0.f, 0.f}};
    {
        const int q4 = r16 >> 2, p4 = lane & 3;
        const unsigned lv = (unsigned)(uintptr_t)(LAS char*)Vs;
        const int row = rstart + 8 * g + q4;
        const unsigned vb = lv + row * 128 + (p4 & 1) * 8;
#pragma unroll
        for (int dh = 0; dh < 2; ++dh) {
            const unsigned ad = vb + (((hh * 4 + 2 * dh + (p4 >> 1)) ^ vswz(row)) << 4);
            s16x4 vl[9], vh[9];
#pragma unroll
            for (int b = 0; b < 9; ++b) { DSRTR(vl[b], ad, b * 4096); DSRTR(vh[b], ad, b * 4096 + 512); }
            asm volatile("s_waitcnt lgkmcnt(0)" ::: "memory");
#pragma unroll
            for (int b = 0; b < 9; ++b) {
                asm volatile("" : "+v"(vl[b])); asm volatile("" : "+v"(vh[b]));
                const bf16x8 vf = __builtin_shufflevector(vl[b], vh[b], 0, 1, 2, 3, 4, 5, 6, 7);
                oa[dh] = __builtin_amdgcn_mfma_f32_16x16x32_bf16(vf, pf[b], oa[dh], 0, 0, 0);
            }
        }
    }
    const __amdgpu_buffer_rsrc_t rsO = __builtin_amdgcn_make_buffer_rsrc((void*)o, 0, S_LEN * D_MODEL * 2, 0x00020000);
    const float inv = __builtin_amdgcn_rcpf(os[0]);
#pragma unroll
    for (int dh = 0; dh < 2; ++dh) {
        u32x2 w; w[0] = pack2(oa[dh][0] * inv, oa[dh][1] * inv); w[1] = pack2(oa[dh][2] * inv, oa[dh][3] * inv);
        __builtin_amdgcn_raw_buffer_store_b64(w, rsO, (qrow * D_MODEL + hp * 64 + hh * 32 + dh * 16 + 4 * g) * 2, 0, 16);
    }
#endif
}

template <typename KF>
static void set_lds(KF kf, int bytes) {
    (void)hipFuncSetAttribute((const void*)kf, hipFuncAttributeMaxDynamicSharedMemorySize, bytes);
}

extern "C" void kernel_launch(void* const* d_in, const int* in_sizes, int n_in, void* d_out, int out_size,
                              void* d_ws, size_t ws_size, hipStream_t stream) {
    const bf16_t* x = (const bf16_t*)d_in[0];
    const bf16_t* wqkv = (const bf16_t*)d_in[1];
    const bf16_t* wo = (const bf16_t*)d_in[2];
    const bf16_t* w1 = (const bf16_t*)d_in[4];
    const bf16_t* b1 = (const bf16_t*)d_in[5];
    const bf16_t* w2 = (const bf16_t*)d_in[6];
    const bf16_t* b2 = (const bf16_t*)d_in[7];
    bf16_t* out = (bf16_t*)d_out;
    char* ws = (char*)d_ws;
    bf16_t* qkv = (bf16_t*)ws;
    bf16_t* h1 = (bf16_t*)ws;
    bf16_t* ob = (bf16_t*)(ws + (size_t)14 * 1048576);
    float* part = (float*)(ws + (size_t)14 * 1048576);
    bf16_t* h = (bf16_t*)(ws + (size_t)22 * 1048576);
    float* xstat = (float*)(ws + (size_t)26 * 1048576);
    float* hstat = xstat + S_LEN * 2;
    float* cs_qkv = hstat + S_LEN * 32;
    float* cs_w1 = cs_qkv + 4 * 3 * D_MODEL;
    unsigned* flags = (unsigned*)(cs_w1 + 4 * D_FF);

    prep_k<<<720, 256, 0, stream>>>(x, wqkv, w1, xstat, cs_qkv, cs_w1, flags);
    {
        auto kf = gemm_k<128, 192, 2, 2, 3, EPI_QKV, 1024, 3072, 8, 4, 1>;
        constexpr int lds = 3 * (128 + 192) * 128 + 4096;
        set_lds(kf, lds);
        kf<<<256, 512, lds, stream>>>(x, wqkv, qkv, nullptr, nullptr, xstat, cs_qkv, (float*)wo, nullptr);
    }
    {
        constexpr int lds = 2 * AT_ROWS * 128;
        set_lds(attn_k, lds);
        attn_k<<<256, 1024, lds, stream>>>(qkv, ob);
    }
    {
        auto kf = gemm_k<128, 64, 2, 2, 3, EPI_WO, 1024, 1024, 4, 8, 1>;
        constexpr int lds = 3 * (128 + 64) * 128 + 4096;
        set_lds(kf, lds);
        kf<<<256, 512, lds, stream>>>(ob, wo, h, x, nullptr, hstat, nullptr, nullptr, nullptr);
    }
    {
        auto kf = gemm_k<128, 224, 2, 2, 3, EPI_MLP1, 1024, 3584, 8, 4, 1>;
        constexpr int lds = 3 * (128 + 256) * 128 + 4096;
        set_lds(kf, lds);
        kf<<<256, 512, lds, stream>>>(h, w1, h1, nullptr, b1, hstat, cs_w1, (float*)w2, nullptr);
    }
    {
        auto kf = gemm_k<128, 128, 2, 2, 4, EPI_MLP2, 3584, 1024, 4, 8, 2>;
        constexpr int lds = 4 * (128 + 128) * 128 + 4096;
        set_lds(kf, lds);
        kf<<<256, 512, lds, stream>>>(h1, w2, out, h, b2, nullptr, nullptr, part, flags);
    }
}
```
